```python
import torch

def _softmax(x, axis=-1):
    return torch.softmax(x, dim=axis)
def _gelu(x):
    return torch.nn.functional.gelu(x, approximate="tanh")
def _silu(x):
    return torch.nn.functional.silu(x)
def _sigmoid(x):
    return torch.sigmoid(x)
def _layernorm(x, axis=-1, eps=1e-5):
    m = x.mean(dim=axis, keepdim=True)
    v = x.var(dim=axis, keepdim=True, unbiased=False)
    return (x - m) / torch.sqrt(v + eps)
def _scatter_add(tgt, idx, src, axis=0):
    return tgt.clone().index_add_(axis, idx, src)
def _segment_sum(src, seg, n):
    out = torch.zeros((n,) + src.shape[1:], dtype=src.dtype)
    return out.index_add_(0, seg, src)
def _pad(x, pw):
    flat = tuple(v for pair in reversed(pw) for v in pair)
    return torch.nn.functional.pad(x, flat)

kernel_name = 'standalone__gqa_block_cached__near_ridge'

def setup_inputs():

    _w = (lambda a: (lambda t: t.to(torch.bfloat16) if t.is_floating_point() else t)(torch.as_tensor(a)))
    B, Sq, D = 16, 48, 448
    Hq, Hkv, Sk, Dh = 16, 8, 48, 256
    assert Hq % Hkv == 0, (Hq, Hkv)
    return {
        "x": _w((lambda *s: torch.randn(*s, dtype=torch.bfloat16) * 0.8)(B, Sq, D)),
        "k_cache": _w((lambda *s: torch.randn(*s, dtype=torch.bfloat16) * 0.8)(B, Hkv, Sk, Dh)),
        "v_cache": _w((lambda *s: torch.randn(*s, dtype=torch.bfloat16) * 0.05)(B, Hkv, Sk, Dh)),
        "wq": _w((lambda *s: torch.randn(*s, dtype=torch.bfloat16) * 0.8)(D, Hq * Dh) * (D ** -0.5)),
        "wo": _w((lambda *s: torch.randn(*s, dtype=torch.bfloat16) * 0.05)(Hq * Dh, D) * ((Hq * Dh) ** -0.5)),
    }

def reference(x, k_cache, v_cache, wq, wo):
    B, Sq, D = x.shape
    Hq, Dh = 16, 256
    Hkv = k_cache.shape[1]
    rep = Hq // Hkv
    q = (lambda x, *d: x.permute(*d))((x @ wq).reshape(B, Sq, Hq, Dh), 0, 2, 1, 3)
    k = (lambda x, n, axis: torch.repeat_interleave(x, n, dim=axis))(k_cache, rep, axis=1)
    v = (lambda x, n, axis: torch.repeat_interleave(x, n, dim=axis))(v_cache, rep, axis=1)
    scores = torch.einsum("bhqd,bhkd->bhqk", q, k) * (Dh ** -0.5)
    p = _softmax(scores, axis=-1)
    o = torch.einsum("bhqk,bhkd->bhqd", p, v)
    return (lambda x, *d: x.permute(*d))(o, 0, 2, 1, 3).reshape(B, Sq, Hq * Dh) @ wo
```

```cpp
#include <hip/hip_runtime.h>
#include <hip/hip_bf16.h>
#include <stdint.h>

typedef unsigned v4u __attribute__((ext_vector_type(4)));
typedef unsigned v2u __attribute__((ext_vector_type(2)));
typedef float f32x4 __attribute__((ext_vector_type(4)));
typedef float f32x2 __attribute__((ext_vector_type(2)));
typedef __bf16 bf16x8_t __attribute__((ext_vector_type(8)));
typedef __bf16 bf16x2_t __attribute__((ext_vector_type(2)));

constexpr int NB = 16, SQ = 48, DM = 448, HQ = 16, HKV = 8, SK = 48, DH = 256;
constexpr int XS_STRIDE = 928;
constexpr int VS_STRIDE = 544;
constexpr int XS_OFF = 0;
constexpr int VS_OFF = XS_OFF + 48 * XS_STRIDE;
constexpr int SP_OFF = VS_OFF + 48 * VS_STRIDE;
constexpr int SMEM_A = SP_OFF + 4 * 9 * 1024;
constexpr int VW_BLK = 43008;
constexpr int VW_R2 = 28672;
constexpr int P_BLK = 4608;
constexpr int P_R2 = 3072;
constexpr size_t P_OFF = (size_t)256 * VW_BLK;

__device__ __forceinline__ unsigned pack_bf16(float a, float b) {
  f32x2 v = {a, b};
  bf16x2_t r = __builtin_convertvector(v, bf16x2_t);
  return __builtin_bit_cast(unsigned, r);
}
__device__ __forceinline__ float round_bf16(float a) {
  __bf16 r = (__bf16)a;
  return (float)r;
}
__device__ __forceinline__ f32x4 mfma16(v4u a, v4u b, f32x4 c) {
  return __builtin_amdgcn_mfma_f32_16x16x32_bf16(__builtin_bit_cast(bf16x8_t, a), __builtin_bit_cast(bf16x8_t, b), c, 0, 0, 0);
}

template <int ROWSTRIDE>
__device__ __forceinline__ void load_stage(v4u (&st)[8], __amdgpu_buffer_rsrc_t rs, int voff, int soff) {
#pragma unroll
  for (int i = 0; i < 8; ++i) st[i] = __builtin_amdgcn_raw_buffer_load_b128(rs, voff, soff + i * ROWSTRIDE, 0);
}

__device__ __forceinline__ v4u wfrag(const v4u (&st)[8], int j) {
  const int p = j >> 1;
  const unsigned sel = (j & 1) ? 0x07060302u : 0x05040100u;
  v4u f;
  f[0] = __builtin_amdgcn_perm(st[1][p], st[0][p], sel);
  f[1] = __builtin_amdgcn_perm(st[3][p], st[2][p], sel);
  f[2] = __builtin_amdgcn_perm(st[5][p], st[4][p], sel);
  f[3] = __builtin_amdgcn_perm(st[7][p], st[6][p], sel);
  return f;
}

__global__ __launch_bounds__(512, 2) void gqa_kernel_a(const unsigned char* __restrict__ x, const unsigned char* __restrict__ kc,
                                                       const unsigned char* __restrict__ vc, const unsigned char* __restrict__ wq,
                                                       const unsigned char* __restrict__ wo, unsigned char* __restrict__ ws) {
  __shared__ __attribute__((aligned(16))) unsigned char smem[SMEM_A];
  const int bid = blockIdx.x;
  const int xcd = bid & 7, bi = bid >> 3;
  const int h = 2 * xcd + (bi & 1), b = bi >> 1;
  const int gk = h >> 1;
  const int tid = threadIdx.x, lane = tid & 63;
  const int w = __builtin_amdgcn_readfirstlane(tid >> 6);
  const int lg = lane >> 4, lc = lane & 15;

  const unsigned char* xb = x + (size_t)b * (SQ * DM * 2);
  const unsigned char* vb = vc + (size_t)(b * HKV + gk) * (SK * DH * 2);
  v4u xr[6], vr[3];
#pragma unroll
  for (int i = 0; i < 6; ++i) {
    int q = tid + 512 * i;
    if (q > 2687) q = 2687;
    xr[i] = *(const v4u*)(xb + q * 16);
  }
#pragma unroll
  for (int i = 0; i < 3; ++i) vr[i] = *(const v4u*)(vb + (tid + 512 * i) * 16);

  v4u st[3][8];
  if (w < 4) {
    const int ch = w & 1, kh = w >> 1;
    const __amdgpu_buffer_rsrc_t rs = __builtin_amdgcn_make_buffer_rsrc((void*)wq, 0, DM * HQ * DH * 2, 0x00020000);
    const int pc = 4 * (lc & 3) + (lc >> 2);
    const int voff = (8 * lg * 4096 + h * 256 + 128 * ch + 8 * pc) * 2;
    int rot = b % 7;
    int ksv[7];
#pragma unroll
    for (int s = 0; s < 7; ++s) {
      int t = s + rot;
      if (t >= 7) t -= 7;
      ksv[s] = 7 * kh + t;
    }
    load_stage<8192>(st[0], rs, voff, ksv[0] * 32 * 8192);
    load_stage<8192>(st[1], rs, voff, ksv[1] * 32 * 8192);

#pragma unroll
    for (int i = 0; i < 6; ++i) {
      int q = tid + 512 * i;
      if (q < 2688) { int r = q / 56, c = q - r * 56; *(v4u*)(smem + XS_OFF + r * XS_STRIDE + c * 16) = xr[i]; }
    }
#pragma unroll
    for (int i = 0; i < 3; ++i) {
      int q = tid + 512 * i;
      *(v4u*)(smem + VS_OFF + (q >> 5) * VS_STRIDE + (q & 31) * 16) = vr[i];
    }
    __syncthreads();

    f32x4 acc[3][8];
#pragma unroll
    for (int qt = 0; qt < 3; ++qt)
#pragma unroll
      for (int j = 0; j < 8; ++j) acc[qt][j] = f32x4{0.f, 0.f, 0.f, 0.f};

    const unsigned char* xl = smem + XS_OFF + lc * XS_STRIDE + lg * 16;
#pragma unroll
    for (int s = 0; s < 7; ++s) {
      if (s + 2 < 7) load_stage<8192>(st[(s + 2) % 3], rs, voff, ksv[s + 2] * 32 * 8192);
      v4u xf[3];
#pragma unroll
      for (int qt = 0; qt < 3; ++qt) xf[qt] = *(const v4u*)(xl + qt * 16 * XS_STRIDE + ksv[s] * 64);
#pragma unroll
      for (int j = 0; j < 8; ++j) {
        v4u wf = wfrag(st[s % 3], j);
#pragma unroll
        for (int qt = 0; qt < 3; ++qt) acc[qt][j] = mfma16(wf, xf[qt], acc[qt][j]);
      }
    }
    const unsigned char* kb = kc + (size_t)(b * HKV + gk) * (SK * DH * 2) + lc * 512 + (128 * ch + 8 * lg) * 2;
    v4u kf[3][4];
#pragma unroll
    for (int kt = 0; kt < 3; ++kt)
#pragma unroll
      for (int r = 0; r < 4; ++r) kf[kt][r] = *(const v4u*)(kb + kt * 16 * 512 + r * 64);

    f32x4 sacc[3][3];
#pragma unroll
    for (int kt = 0; kt < 3; ++kt)
#pragma unroll
      for (int qt = 0; qt < 3; ++qt) sacc[kt][qt] = f32x4{0.f, 0.f, 0.f, 0.f};
#pragma unroll
    for (int r = 0; r < 4; ++r) {
#pragma unroll
      for (int qt = 0; qt < 3; ++qt) {
        v4u qf;
        qf[0] = pack_bf16(acc[qt][0][r], acc[qt][1][r]);
        qf[1] = pack_bf16(acc[qt][2][r], acc[qt][3][r]);
        qf[2] = pack_bf16(acc[qt][4][r], acc[qt][5][r]);
        qf[3] = pack_bf16(acc[qt][6][r], acc[qt][7][r]);
#pragma unroll
        for (int kt = 0; kt < 3; ++kt) sacc[kt][qt] = mfma16(kf[kt][r], qf, sacc[kt][qt]);
      }
    }
#pragma unroll
    for (int kt = 0; kt < 3; ++kt)
#pragma unroll
      for (int qt = 0; qt < 3; ++qt) *(f32x4*)(smem + SP_OFF + ((w * 9 + kt * 3 + qt) * 64 + lane) * 16) = sacc[kt][qt];
    __syncthreads();
    if (w < 3) {
      const int qt = w;
      f32x4 s[3];
#pragma unroll
      for (int kt = 0; kt < 3; ++kt) {
        f32x4 a = *(const f32x4*)(smem + SP_OFF + ((0 * 9 + kt * 3 + qt) * 64 + lane) * 16);
        f32x4 b1 = *(const f32x4*)(smem + SP_OFF + ((1 * 9 + kt * 3 + qt) * 64 + lane) * 16);
        f32x4 c = *(const f32x4*)(smem + SP_OFF + ((2 * 9 + kt * 3 + qt) * 64 + lane) * 16);
        f32x4 d = *(const f32x4*)(smem + SP_OFF + ((3 * 9 + kt * 3 + qt) * 64 + lane) * 16);
        s[kt] = (a + b1) + (c + d);
      }
      float m = -3.0e38f;
#pragma unroll
      for (int kt = 0; kt < 3; ++kt)
#pragma unroll
        for (int r = 0; r < 4; ++r) {
          float t = round_bf16(s[kt][r]) * 0.0625f;
          s[kt][r] = t;
          m = fmaxf(m, t);
        }
      m = fmaxf(m, __shfl_xor(m, 16));
      m = fmaxf(m, __shfl_xor(m, 32));
      float sum = 0.f;
#pragma unroll
      for (int kt = 0; kt < 3; ++kt)
#pragma unroll
        for (int r = 0; r < 4; ++r) {
          float e = __expf(s[kt][r] - m);
          s[kt][r] = e;
          sum += e;
        }
      sum += __shfl_xor(sum, 16);
      sum += __shfl_xor(sum, 32);
      const float inv = 1.0f / sum;
      v4u p1;
      v2u p2;
      p1[0] = pack_bf16(s[0][0] * inv, s[0][1] * inv);
      p1[1] = pack_bf16(s[0][2] * inv, s[0][3] * inv);
      p1[2] = pack_bf16(s[1][0] * inv, s[1][1] * inv);
      p1[3] = pack_bf16(s[1][2] * inv, s[1][3] * inv);
      p2[0] = pack_bf16(s[2][0] * inv, s[2][1] * inv);
      p2[1] = pack_bf16(s[2][2] * inv, s[2][3] * inv);
      unsigned char* pb = ws + P_OFF + (size_t)(b * 16 + h) * P_BLK;
      *(v4u*)(pb + qt * 1024 + lane * 16) = p1;
      *(v2u*)(pb + P_R2 + qt * 512 + lane * 8) = p2;
    }
  } else {
    const int cw = w - 4;
    const __amdgpu_buffer_rsrc_t rs = __builtin_amdgcn_make_buffer_rsrc((void*)wo, 0, DM * HQ * DH * 2, 0x00020000);
    const int lcc = lc < 14 ? lc : 13;
    const int voff = ((h * 256 + 8 * lg) * 448 + 112 * cw + 8 * lcc) * 2;
    const int rot = b & 7;
    int ksv[8];
#pragma unroll
    for (int s = 0; s < 8; ++s) ksv[s] = (s + rot) & 7;
    load_stage<896>(st[0], rs, voff, ksv[0] * 32 * 896);
    load_stage<896>(st[1], rs, voff, ksv[1] * 32 * 896);

#pragma unroll
    for (int i = 0; i < 6; ++i) {
      int q = tid + 512 * i;
      if (q < 2688) { int r = q / 56, c = q - r * 56; *(v4u*)(smem + XS_OFF + r * XS_STRIDE + c * 16) = xr[i]; }
    }
#pragma unroll
    for (int i = 0; i < 3; ++i) {
      int q = tid + 512 * i;
      *(v4u*)(smem + VS_OFF + (q >> 5) * VS_STRIDE + (q & 31) * 16) = vr[i];
    }
    __syncthreads();

    f32x4 acc[3][8];
#pragma unroll
    for (int kt = 0; kt < 3; ++kt)
#pragma unroll
      for (int j = 0; j < 8; ++j) acc[kt][j] = f32x4{0.f, 0.f, 0.f, 0.f};

    const unsigned char* vl = smem + VS_OFF + lc * VS_STRIDE + lg * 16;
#pragma unroll
    for (int s = 0; s < 8; ++s) {
      if (s + 2 < 8) load_stage<896>(st[(s + 2) % 3], rs, voff, ksv[s + 2] * 32 * 896);
      v4u vf[3];
#pragma unroll
      for (int kt = 0; kt < 3; ++kt) vf[kt] = *(const v4u*)(vl + kt * 16 * VS_STRIDE + ksv[s] * 64);
#pragma unroll
      for (int j = 0; j < 8; ++j) {
        v4u wf = wfrag(st[s % 3], j);
#pragma unroll
        for (int kt = 0; kt < 3; ++kt) acc[kt][j] = mfma16(vf[kt], wf, acc[kt][j]);
      }
    }
    if (lc < 14) {
      unsigned char* ob = ws + (size_t)(b * 16 + h) * VW_BLK;
      const int nt = 7 * cw + (lc >> 1);
      unsigned char* o1 = ob + nt * 1024 + lg * 256 + (lc & 1) * 128;
      unsigned char* o2 = ob + VW_R2 + nt * 512 + lg * 128 + (lc & 1) * 64;
#pragma unroll
      for (int j = 0; j < 8; ++j) {
        v4u o;
        o[0] = pack_bf16(acc[0][j][0], acc[0][j][1]);
        o[1] = pack_bf16(acc[0][j][2], acc[0][j][3]);
        o[2] = pack_bf16(acc[1][j][0], acc[1][j][1]);
        o[3] = pack_bf16(acc[1][j][2], acc[1][j][3]);
        *(v4u*)(o1 + j * 16) = o;
      }
#pragma unroll
      for (int j = 0; j < 8; j += 2) {
        v4u o;
        o[0] = pack_bf16(acc[2][j][0], acc[2][j][1]);
        o[1] = pack_bf16(acc[2][j][2], acc[2][j][3]);
        o[2] = pack_bf16(acc[2][j + 1][0], acc[2][j + 1][1]);
        o[3] = pack_bf16(acc[2][j + 1][2], acc[2][j + 1][3]);
        *(v4u*)(o2 + j * 8) = o;
      }
    }
    __syncthreads();
  }
}

__global__ __launch_bounds__(512) void gqa_kernel_b(const unsigned char* __restrict__ ws, unsigned short* __restrict__ out) {
  __shared__ __attribute__((aligned(16))) unsigned char smem[8 * 6 * 1024];
  const int bid = blockIdx.x;
  const int xcd = bid & 7, bi = bid >> 3;
  const int b = xcd + 8 * (bi / 14), m = bi % 14;
  const int tid = threadIdx.x, lane = tid & 63;
  const int w = __builtin_amdgcn_readfirstlane(tid >> 6);

  v4u p1[2][3], v1[2][2];
  v2u p2[2][3], v2[2][2];
#pragma unroll
  for (int hh = 0; hh < 2; ++hh) {
    const int h = 2 * w + hh;
    const unsigned char* pb = ws + P_OFF + (size_t)(b * 16 + h) * P_BLK;
    const unsigned char* vb = ws + (size_t)(b * 16 + h) * VW_BLK;
#pragma unroll
    for (int ct = 0; ct < 2; ++ct) {
      v1[hh][ct] = *(const v4u*)(vb + (2 * m + ct) * 1024 + lane * 16);
      v2[hh][ct] = *(const v2u*)(vb + VW_R2 + (2 * m + ct) * 512 + lane * 8);
    }
#pragma unroll
    for (int qt = 0; qt < 3; ++qt) {
      p1[hh][qt] = *(const v4u*)(pb + qt * 1024 + lane * 16);
      p2[hh][qt] = *(const v2u*)(pb + P_R2 + qt * 512 + lane * 8);
    }
  }
  f32x4 acc[3][2];
#pragma unroll
  for (int qt = 0; qt < 3; ++qt)
#pragma unroll
    for (int ct = 0; ct < 2; ++ct) {
      f32x4 a = f32x4{0.f, 0.f, 0.f, 0.f};
      a = mfma16(p1[0][qt], v1[0][ct], a);
      a = mfma16(p1[1][qt], v1[1][ct], a);
      v4u pa = {p2[0][qt][0], p2[0][qt][1], p2[1][qt][0], p2[1][qt][1]};
      v4u va = {v2[0][ct][0], v2[0][ct][1], v2[1][ct][0], v2[1][ct][1]};
      a = mfma16(pa, va, a);
      acc[qt][ct] = a;
    }
#pragma unroll
  for (int qt = 0; qt < 3; ++qt)
#pragma unroll
    for (int ct = 0; ct < 2; ++ct) *(f32x4*)(smem + ((w * 6 + qt * 2 + ct) * 64 + lane) * 16) = acc[qt][ct];
  __syncthreads();
  if (w < 6) {
    const int qt = w >> 1, ct = w & 1;
    f32x4 s = f32x4{0.f, 0.f, 0.f, 0.f};
#pragma unroll
    for (int wv = 0; wv < 8; ++wv) s += *(const f32x4*)(smem + ((wv * 6 + w) * 64 + lane) * 16);
    const int lg = lane >> 4, lc = lane & 15;
    unsigned short* ob = out + ((size_t)b * SQ + 16 * qt + 4 * lg) * DM + 32 * m + 16 * ct + lc;
#pragma unroll
    for (int r = 0; r < 4; ++r) {
      __bf16 v = (__bf16)s[r];
      ob[r * DM] = __builtin_bit_cast(unsigned short, v);
    }
  }
}

extern "C" void kernel_launch(void* const* d_in, const int* in_sizes, int n_in, void* d_out, int out_size, void* d_ws,
                              size_t ws_size, hipStream_t stream) {
  const unsigned char* x = (const unsigned char*)d_in[0];
  const unsigned char* kc = (const unsigned char*)d_in[1];
  const unsigned char* vc = (const unsigned char*)d_in[2];
  const unsigned char* wq = (const unsigned char*)d_in[3];
  const unsigned char* wo = (const unsigned char*)d_in[4];
  unsigned char* ws = (unsigned char*)d_ws;
  gqa_kernel_a<<<256, 512, 0, stream>>>(x, kc, vc, wq, wo, ws);
  gqa_kernel_b<<<224, 512, 0, stream>>>(ws, (unsigned short*)d_out);
}
```

```cpp
#include <hip/hip_runtime.h>
#include <hip/hip_bf16.h>
#include <stdint.h>

typedef unsigned v4u __attribute__((ext_vector_type(4)));
typedef unsigned v2u __attribute__((ext_vector_type(2)));
typedef short v4s __attribute__((ext_vector_type(4)));
typedef float f32x4 __attribute__((ext_vector_type(4)));
typedef float f32x2 __attribute__((ext_vector_type(2)));
typedef __bf16 bf16x8_t __attribute__((ext_vector_type(8)));
typedef __bf16 bf16x2_t __attribute__((ext_vector_type(2)));
#define LDS_AS __attribute__((address_space(3)))

constexpr int NB = 16, SQ = 48, DM = 448, HQ = 16, HKV = 8, SK = 48, DH = 256;
constexpr int SMEM_A = 163840;
constexpr int QSLOT = 45056;
constexpr int VSLOT = 57344;
constexpr int VIMG = 49152;
constexpr int VW_BLK = 43008;
constexpr int VW_R2 = 28672;
constexpr int P_BLK = 4608;
constexpr int P_R2 = 3072;
constexpr size_t P_OFF = (size_t)256 * VW_BLK;

#define TS(i)
#define TSDUMP(WSP)
#define STAMP_BEGIN
#define STAMP_END(K, WSP)

#define AUXW 2
#define AUXX 0
#define AUXKV 2
#define AUXST 0

__device__ __forceinline__ unsigned pack_bf16(float a, float b) {
  f32x2 v = {a, b};
  bf16x2_t r = __builtin_convertvector(v, bf16x2_t);
  return __builtin_bit_cast(unsigned, r);
}
__device__ __forceinline__ float round_bf16(float a) {
  __bf16 r = (__bf16)a;
  return (float)r;
}
__device__ __forceinline__ f32x4 mfma16(v4u a, v4u b, f32x4 c) {
  return __builtin_amdgcn_mfma_f32_16x16x32_bf16(__builtin_bit_cast(bf16x8_t, a), __builtin_bit_cast(bf16x8_t, b), c, 0, 0, 0);
}
__device__ __forceinline__ v2u tr_read(const unsigned char* p) {
  v4s r = __builtin_amdgcn_ds_read_tr16_b64_v4i16((LDS_AS v4s*)p);
  return __builtin_bit_cast(v2u, r);
}
template <int AUX>
__device__ __forceinline__ void dma16(__amdgpu_buffer_rsrc_t rs, unsigned ldsoff, int voff, int soff) {
  if constexpr (AUX == 2)
    asm volatile("s_nop 4\n\ts_mov_b32 m0, %0\n\ts_nop 0\n\tbuffer_load_dwordx4 %1, %2, %3 offen nt lds" :: "s"(ldsoff), "v"(voff), "s"(rs), "s"(soff) : "m0", "memory");
  else
    asm volatile("s_nop 4\n\ts_mov_b32 m0, %0\n\ts_nop 0\n\tbuffer_load_dwordx4 %1, %2, %3 offen lds" :: "s"(ldsoff), "v"(voff), "s"(rs), "s"(soff) : "m0", "memory");
}
#define DMA16(RS, LDSOFF, VOFF, SOFF, AUX) dma16<AUX>((RS), lds0 + (LDSOFF), (VOFF), (SOFF))
#define WAITV(n) asm volatile("s_waitcnt vmcnt(" #n ")" ::: "memory")
#define RBAR() { asm volatile("s_waitcnt lgkmcnt(0)" ::: "memory"); __builtin_amdgcn_sched_barrier(0); __builtin_amdgcn_s_barrier(); __builtin_amdgcn_sched_barrier(0); }

__device__ __forceinline__ constexpr int qslot(int c) { return (c % 3 == 0) ? 0 : ((c % 3 == 2) ? QSLOT : 2 * QSLOT); }
constexpr int KIMG = 2 * QSLOT;

__global__ __launch_bounds__(640, 3) void gqa_kernel_a(const unsigned char* __restrict__ x, const unsigned char* __restrict__ kc,
                                                       const unsigned char* __restrict__ vc, const unsigned char* __restrict__ wq,
                                                       const unsigned char* __restrict__ wo, unsigned char* __restrict__ ws) {
  __shared__ __attribute__((aligned(16))) unsigned char smem[SMEM_A];
  STAMP_BEGIN
  TS(0)
  const int bid = blockIdx.x;
  const int xcd = bid & 7, bi = bid >> 3;
  const int type = bi & 1, h = 2 * xcd + ((bi >> 1) & 1), bp = bi >> 2;
  const int gk = h >> 1;
  const int tid = threadIdx.x, lane = tid & 63;
  const int w = __builtin_amdgcn_readfirstlane(tid >> 6);
  const int lg = lane >> 4, lc = lane & 15;
  const int hi = lane >> 5;
  const int q4 = (lane & 15) >> 2, p4 = lane & 3;
  const int bb = (w >> 2) & 1;
  const int cq = w & 3;
  const unsigned lds0 = (unsigned)(size_t)(LDS_AS unsigned char*)smem;

  if (type == 0) {
    if (w >= 8) {
      const int ld = w - 8;
      const __amdgpu_buffer_rsrc_t rsw = __builtin_amdgcn_make_buffer_rsrc((void*)wq, 0, DM * HQ * DH * 2, 0x00020000);
      const __amdgpu_buffer_rsrc_t rsx = __builtin_amdgcn_make_buffer_rsrc((void*)(x + (size_t)bp * (2 * SQ * DM * 2)), 0, 2 * SQ * DM * 2, 0x00020000);
      const __amdgpu_buffer_rsrc_t rsk = __builtin_amdgcn_make_buffer_rsrc((void*)kc, 0, NB * HKV * SK * DH * 2, 0x00020000);
      int wv[4], xv[2];
#pragma unroll
      for (int v = 0; v < 4; ++v) {
        const int sw = (2 * (v & 1) + hi) | ((v >> 1) << 2);
        wv[v] = hi * 8192 + h * 512 + (((lane & 31) ^ (sw << 1)) * 16);
      }
#pragma unroll
      for (int v = 0; v < 2; ++v) xv[v] = (lane >> 3) * 896 + (((lane & 7) ^ ((4 * v + (lane >> 4)) & 7)) * 16);
      auto issue = [&](int c) {
        const unsigned slot = qslot(c);
#pragma unroll
        for (int i = 0; i < 6; ++i) DMA16(rsx, slot + 32768 + (6 * ld + i) * 1024, xv[i & 1], (6 * ld + i) * 8 * 896 + 128 * c, AUXX);
#pragma unroll
        for (int i = 0; i < 16; ++i)
          DMA16(rsw, slot + ld * 16384 + i * 1024, wv[(i & 1) | (((i >> 2) & 1) << 1)], ((2 * c + ld) * 32 + 2 * i) * 8192, AUXW);
      };
      issue(0);
      issue(1);
      __builtin_amdgcn_sched_barrier(0);
      unsigned tv[4];
      {
#pragma unroll
        for (int i = 0; i < 2; ++i) {
          const int idx = lane + 64 * i;
          const int L = 8 * (ld * 80 + idx) + bp;
          const int off = idx < 80 ? (128 + (L >> 2)) * 8192 + h * 512 + (L & 3) * 128 : 0x7ffffff0;
          tv[i] = __builtin_amdgcn_raw_buffer_load_b32(rsw, off, 0, AUXW);
        }
#pragma unroll
        for (int i = 0; i < 2; ++i) {
          const int idx = lane + 64 * i;
          const int off = idx < 96 ? ((2 * bp + ld) * HKV + gk) * (SK * DH * 2) + (2 * idx + (h & 1)) * 128 : 0x7ffffff0;
          tv[2 + i] = __builtin_amdgcn_raw_buffer_load_b32(rsk, off, 0, AUXKV);
        }
      }
      __builtin_amdgcn_sched_barrier(0);
#pragma unroll
      for (int c = 0; c < 7; ++c) {
        if (c == 0) { WAITV(26); } else if (c < 6) { WAITV(22); } else { WAITV(24); }
        RBAR()
        if (c + 2 < 7) issue(c + 2);
        if (c == 5) {
          const int k_voff = hi * 512;
#pragma unroll
          for (int i = 0; i < 24; ++i) {
            const int kvo = k_voff + (((lane & 31) ^ ((2 * i + hi) & 15)) * 16);
            DMA16(rsk, KIMG + ld * 24576 + i * 1024, kvo, ((2 * bp + ld) * HKV + gk) * (SK * DH * 2) + i * 1024, AUXKV);
          }
        }
      }
      WAITV(0);
      RBAR()
      RBAR()
      asm volatile("" :: "v"(tv[0]), "v"(tv[1]), "v"(tv[2]), "v"(tv[3]));
    } else {
      f32x4 acc[3][4];
#pragma unroll
      for (int qt = 0; qt < 3; ++qt)
#pragma unroll
        for (int j = 0; j < 4; ++j) acc[qt][j] = f32x4{0.f, 0.f, 0.f, 0.f};
      const int xrd = 32768 + (48 * bb + lc) * 128 + ((lg ^ ((lc >> 1) & 7)) * 16);
      const int srd = q4 | ((lg & 1) << 2);
      const int wrd = (8 * lg + q4) * 512 + (((8 * cq + (p4 >> 1)) ^ (srd << 1)) * 16) + (p4 & 1) * 8;
      TS(1)
#pragma unroll
      for (int c = 0; c < 7; ++c) {
        RBAR()
        const unsigned char* slot = smem + qslot(c);
#pragma unroll
        for (int ss = 0; ss < 2; ++ss) {
          v4u xf[3];
#pragma unroll
          for (int qt = 0; qt < 3; ++qt) xf[qt] = *(const v4u*)(slot + ((xrd ^ (64 * ss)) + qt * 2048));
#pragma unroll
          for (int j = 0; j < 4; ++j) {
            v2u a = tr_read(slot + ss * 16384 + (wrd ^ (32 * j)));
            v2u b = tr_read(slot + ss * 16384 + (wrd ^ (32 * j)) + 2048);
            v4u wf = {a[0], a[1], b[0], b[1]};
#pragma unroll
            for (int qt = 0; qt < 3; ++qt) acc[qt][j] = mfma16(wf, xf[qt], acc[qt][j]);
          }
        }
        __builtin_amdgcn_sched_barrier(0);
        TS(2 + c)
      }
      RBAR()
      f32x4 sacc[3][3];
      {
        const unsigned char* kl = smem + KIMG + (48 * bb + lc) * 512 + (lg & 1) * 8;
        v4u ka[3][2];
#pragma unroll
        for (int kt = 0; kt < 3; ++kt)
#pragma unroll
          for (int f = 0; f < 2; ++f) {
            v2u a = *(const v2u*)(kl + kt * 8192 + (((8 * cq + 4 * f + (lg >> 1)) ^ lc) * 16));
            v2u b = *(const v2u*)(kl + kt * 8192 + (((8 * cq + 4 * f + 2 + (lg >> 1)) ^ lc) * 16));
            ka[kt][f] = v4u{a[0], a[1], b[0], b[1]};
          }
#pragma unroll
        for (int qt = 0; qt < 3; ++qt) {
#pragma unroll
          for (int kt = 0; kt < 3; ++kt) sacc[kt][qt] = f32x4{0.f, 0.f, 0.f, 0.f};
#pragma unroll
          for (int f = 0; f < 2; ++f) {
            v4u qf;
            qf[0] = pack_bf16(acc[qt][2 * f][0], acc[qt][2 * f][1]);
            qf[1] = pack_bf16(acc[qt][2 * f][2], acc[qt][2 * f][3]);
            qf[2] = pack_bf16(acc[qt][2 * f + 1][0], acc[qt][2 * f + 1][1]);
            qf[3] = pack_bf16(acc[qt][2 * f + 1][2], acc[qt][2 * f + 1][3]);
#pragma unroll
            for (int kt = 0; kt < 3; ++kt) sacc[kt][qt] = mfma16(ka[kt][f], qf, sacc[kt][qt]);
          }
        }
      }
      TS(9)
      unsigned char* sp = smem;
#pragma unroll
      for (int kt = 0; kt < 3; ++kt)
#pragma unroll
        for (int qt = 0; qt < 3; ++qt) *(f32x4*)(sp + ((w * 9 + qt * 3 + kt) * 64 + lane) * 16) = sacc[kt][qt];
      TS(10)
      RBAR()
      TS(11)
      if (w < 6) {
        const int b2 = w >= 3 ? 1 : 0, qt = w - 3 * b2;
        f32x4 s[3];
#pragma unroll
        for (int kt = 0; kt < 3; ++kt) {
          f32x4 a = *(const f32x4*)(sp + (((4 * b2 + 0) * 9 + qt * 3 + kt) * 64 + lane) * 16);
#pragma unroll
          for (int wv = 1; wv < 4; ++wv) a += *(const f32x4*)(sp + (((4 * b2 + wv) * 9 + qt * 3 + kt) * 64 + lane) * 16);
          s[kt] = a;
        }
        float m = -3.0e38f;
#pragma unroll
        for (int kt = 0; kt < 3; ++kt)
#pragma unroll
          for (int r = 0; r < 4; ++r) {
            float t = round_bf16(s[kt][r]) * 0.0625f;
            s[kt][r] = t;
            m = fmaxf(m, t);
          }
        m = fmaxf(m, __shfl_xor(m, 16));
        m = fmaxf(m, __shfl_xor(m, 32));
        float sum = 0.f;
#pragma unroll
        for (int kt = 0; kt < 3; ++kt)
#pragma unroll
          for (int r = 0; r < 4; ++r) {
            float e = __expf(s[kt][r] - m);
            s[kt][r] = e;
            sum += e;
          }
        sum += __shfl_xor(sum, 16);
        sum += __shfl_xor(sum, 32);
        const float inv = 1.0f / sum;
        v4u p1;
        v2u p2;
        p1[0] = pack_bf16(s[0][0] * inv, s[0][1] * inv);
        p1[1] = pack_bf16(s[0][2] * inv, s[0][3] * inv);
        p1[2] = pack_bf16(s[1][0] * inv, s[1][1] * inv);
        p1[3] = pack_bf16(s[1][2] * inv, s[1][3] * inv);
        p2[0] = pack_bf16(s[2][0] * inv, s[2][1] * inv);
        p2[1] = pack_bf16(s[2][2] * inv, s[2][3] * inv);
        unsigned char* pb = ws + P_OFF + (size_t)((2 * bp + b2) * 16 + h) * P_BLK;
        *(v4u*)(pb + qt * 1024 + lane * 16) = p1;
        *(v2u*)(pb + P_R2 + qt * 512 + lane * 8) = p2;
      }
    }
  } else {
    if (w >= 8) {
      const int ld = w - 8;
      const __amdgpu_buffer_rsrc_t rs = __builtin_amdgcn_make_buffer_rsrc((void*)wo, 0, DM * HQ * DH * 2, 0x00020000);
      const __amdgpu_buffer_rsrc_t rsv = __builtin_amdgcn_make_buffer_rsrc((void*)vc, 0, NB * HKV * SK * DH * 2, 0x00020000);
#pragma unroll
      for (int i = 0; i < 24; ++i) {
        const int vvo = hi * 512 + (((lane & 31) ^ ((2 * i + hi) & 15)) * 16);
        DMA16(rsv, ld * 24576 + i * 1024, vvo, ((2 * bp + ld) * HKV + gk) * (SK * DH * 2) + i * 1024, AUXKV);
      }
      int wo_voff[2][7];
#pragma unroll
      for (int a = 0; a < 2; ++a)
#pragma unroll
        for (int r = 0; r < 7; ++r) {
          const int P = 1024 * r + 16 * lane;
          const int row = P / 896;
          const int phys = (P - 896 * row) >> 4;
          const int sp_ = ((row >> 1) & 1) | (a << 1);
          wo_voff[a][r] = row * 896 + ((phys ^ (sp_ << 1)) * 16);
        }
      auto issue = [&](int c) {
        const unsigned slot = VIMG + (c & 1) * VSLOT + ld * 28672;
#pragma unroll
        for (int a = 0; a < 4; ++a)
#pragma unroll
          for (int r = 0; r < 7; ++r) DMA16(rs, slot + (7 * a + r) * 1024, wo_voff[a & 1][r], (h * 256 + 64 * c + 32 * ld + 8 * a) * 896, AUXW);
      };
      issue(0);
      __builtin_amdgcn_sched_barrier(0);
      WAITV(34);
      issue(1);
      __builtin_amdgcn_sched_barrier(0);
      unsigned tv;
      {
        const int L = 8 * (ld * 56 + lane) + bp;
        const int off = lane < 56 ? (h * 256 + 128) * 896 + L * 128 : 0x7ffffff0;
        tv = __builtin_amdgcn_raw_buffer_load_b32(rs, off, 0, AUXW);
      }
      __builtin_amdgcn_sched_barrier(0);
#pragma unroll
      for (int c = 0; c < 4; ++c) {
        if (c == 0) { WAITV(29); } else { WAITV(0); }
        RBAR()
        if (c >= 1 && c + 1 < 4) issue(c + 1);
      }
      asm volatile("" :: "v"(tv));
    } else {
      const int cw = cq;
      f32x4 acc[3][7];
#pragma unroll
      for (int kt = 0; kt < 3; ++kt)
#pragma unroll
        for (int ct = 0; ct < 7; ++ct) acc[kt][ct] = f32x4{0.f, 0.f, 0.f, 0.f};
      const int vrd = (48 * bb + lc) * 512;
      const int sp2 = ((q4 >> 1) & 1) | ((lg & 1) << 1);
      const int wrow = (8 * lg + q4) * 896 + (p4 & 1) * 8;
      TS(1)
#pragma unroll
      for (int c = 0; c < 4; ++c) {
        RBAR()
        const unsigned char* slot = smem + VIMG + (c & 1) * VSLOT;
#pragma unroll
        for (int ss = 0; ss < 2; ++ss) {
          v4u vf[3];
#pragma unroll
          for (int kt = 0; kt < 3; ++kt) vf[kt] = *(const v4u*)(smem + vrd + kt * 8192 + (((4 * (2 * c + ss) + lg) ^ lc) * 16));
#pragma unroll
          for (int ct = 0; ct < 7; ++ct) {
            const int phys = (14 * cw + 2 * ct + (p4 >> 1)) ^ (sp2 << 1);
            v2u a = tr_read(slot + ss * 28672 + wrow + phys * 16);
            v2u b = tr_read(slot + ss * 28672 + wrow + phys * 16 + 3584);
            v4u wf = {a[0], a[1], b[0], b[1]};
#pragma unroll
            for (int kt = 0; kt < 3; ++kt) acc[kt][ct] = mfma16(vf[kt], wf, acc[kt][ct]);
          }
        }
        __builtin_amdgcn_sched_barrier(0);
        TS(2 + c)
      }
      const __amdgpu_buffer_rsrc_t rso = __builtin_amdgcn_make_buffer_rsrc((void*)ws, 0, 256 * VW_BLK, 0x00020000);
      {
        const int ob = ((2 * bp + bb) * 16 + h) * VW_BLK;
        const int o1 = ob + 7 * cw * 1024 + lane * 16;
        const int o2 = ob + VW_R2 + 7 * cw * 512 + lane * 8;
#pragma unroll
        for (int ct = 0; ct < 7; ++ct) {
          v4u o;
          o[0] = pack_bf16(acc[0][ct][0], acc[0][ct][1]);
          o[1] = pack_bf16(acc[0][ct][2], acc[0][ct][3]);
          o[2] = pack_bf16(acc[1][ct][0], acc[1][ct][1]);
          o[3] = pack_bf16(acc[1][ct][2], acc[1][ct][3]);
          __builtin_amdgcn_raw_buffer_store_b128(o, rso, o1 + ct * 1024, 0, AUXST);
          v2u o3;
          o3[0] = pack_bf16(acc[2][ct][0], acc[2][ct][1]);
          o3[1] = pack_bf16(acc[2][ct][2], acc[2][ct][3]);
          __builtin_amdgcn_raw_buffer_store_b64(o3, rso, o2 + ct * 512, 0, AUXST);
        }
      }
      TS(10)
    }
  }
  TS(12)
  TSDUMP(ws)
  STAMP_END(0, ws)
}

__global__ __launch_bounds__(512) void gqa_kernel_b(const unsigned char* __restrict__ ws, unsigned char* __restrict__ out) {
  __shared__ __attribute__((aligned(16))) float red[8 * 48 * 36];
  STAMP_BEGIN
  const int bid = blockIdx.x;
  const int xcd = bid & 7, bi = bid >> 3;
  const int b = xcd + 8 * (bi / 14), m = bi % 14;
  const int tid = threadIdx.x, lane = tid & 63;
  const int w = __builtin_amdgcn_readfirstlane(tid >> 6);
  const int lg = lane >> 4, lc = lane & 15;

  v4u p1[2][3], v1[2][2];
  v2u p2[2][3], v2[2][2];
#pragma unroll
  for (int hh = 0; hh < 2; ++hh) {
    const int h = 2 * w + hh;
    const unsigned char* pb = ws + P_OFF + (size_t)(b * 16 + h) * P_BLK;
    const unsigned char* vb = ws + (size_t)(b * 16 + h) * VW_BLK;
#pragma unroll
    for (int ct = 0; ct < 2; ++ct) {
      v1[hh][ct] = *(const v4u*)(vb + (2 * m + ct) * 1024 + lane * 16);
      v2[hh][ct] = *(const v2u*)(vb + VW_R2 + (2 * m + ct) * 512 + lane * 8);
    }
#pragma unroll
    for (int qt = 0; qt < 3; ++qt) {
      p1[hh][qt] = *(const v4u*)(pb + qt * 1024 + lane * 16);
      p2[hh][qt] = *(const v2u*)(pb + P_R2 + qt * 512 + lane * 8);
    }
  }
#pragma unroll
  for (int qt = 0; qt < 3; ++qt)
#pragma unroll
    for (int ct = 0; ct < 2; ++ct) {
      f32x4 a = f32x4{0.f, 0.f, 0.f, 0.f};
      a = mfma16(p1[0][qt], v1[0][ct], a);
      a = mfma16(p1[1][qt], v1[1][ct], a);
      v4u pa = {p2[0][qt][0], p2[0][qt][1], p2[1][qt][0], p2[1][qt][1]};
      v4u va = {v2[0][ct][0], v2[0][ct][1], v2[1][ct][0], v2[1][ct][1]};
      a = mfma16(pa, va, a);
#pragma unroll
      for (int r = 0; r < 4; ++r) red[(w * 48 + 16 * qt + 4 * lg + r) * 36 + 16 * ct + lc] = a[r];
    }
  __syncthreads();
  if (tid < 384) {
    const int row = tid >> 3, c4 = tid & 7;
    f32x4 s = *(const f32x4*)(red + row * 36 + 4 * c4);
#pragma unroll
    for (int wv = 1; wv < 8; ++wv) s += *(const f32x4*)(red + (wv * 48 + row) * 36 + 4 * c4);
    v2u o = {pack_bf16(s[0], s[1]), pack_bf16(s[2], s[3])};
    *(v2u*)(out + (((size_t)b * SQ + row) * DM + 32 * m + 4 * c4) * 2) = o;
  }
  STAMP_END(1, ws)
}

extern "C" void kernel_launch(void* const* d_in, const int* in_sizes, int n_in, void* d_out, int out_size, void* d_ws,
                              size_t ws_size, hipStream_t stream) {
  const unsigned char* x = (const unsigned char*)d_in[0];
  const unsigned char* kc = (const unsigned char*)d_in[1];
  const unsigned char* vc = (const unsigned char*)d_in[2];
  const unsigned char* wq = (const unsigned char*)d_in[3];
  const unsigned char* wo = (const unsigned char*)d_in[4];
  unsigned char* ws = (unsigned char*)d_ws;
  gqa_kernel_a<<<256, 640, 0, stream>>>(x, kc, vc, wq, wo, ws);
  gqa_kernel_b<<<224, 512, 0, stream>>>(ws, (unsigned char*)d_out);
}
```

```cpp
#include <hip/hip_runtime.h>
#include <hip/hip_bf16.h>
#include <stdint.h>

typedef unsigned v4u __attribute__((ext_vector_type(4)));
typedef unsigned v2u __attribute__((ext_vector_type(2)));
typedef short v4s __attribute__((ext_vector_type(4)));
typedef float f32x4 __attribute__((ext_vector_type(4)));
typedef float f32x2 __attribute__((ext_vector_type(2)));
typedef __bf16 bf16x8_t __attribute__((ext_vector_type(8)));
typedef __bf16 bf16x2_t __attribute__((ext_vector_type(2)));
#define LDS_AS __attribute__((address_space(3)))

constexpr int NB = 16, SQ = 48, DM = 448, HQ = 16, HKV = 8, SK = 48, DH = 256;
constexpr int SMEM_A = 163840;
constexpr int QSLOT = 45056;
constexpr int VSLOT = 57344;
constexpr int VIMG = 49152;
constexpr int VW_BLK = 43008;
constexpr int VW_R2 = 28672;
constexpr int P_BLK = 4608;
constexpr int P_R2 = 3072;
constexpr size_t P_OFF = (size_t)256 * VW_BLK;

#define TS(i)
#define TSDUMP(WSP)
#define STAMP_BEGIN
#define STAMP_END(K, WSP)

#define AUXW 0
#define AUXX 0
#define AUXKV 0
#define AUXT 2
#define TOUCH 1
#define ROT 1
#define AUXST 16

__device__ __forceinline__ unsigned pack_bf16(float a, float b) {
  f32x2 v = {a, b};
  bf16x2_t r = __builtin_convertvector(v, bf16x2_t);
  return __builtin_bit_cast(unsigned, r);
}
__device__ __forceinline__ float round_bf16(float a) {
  __bf16 r = (__bf16)a;
  return (float)r;
}
__device__ __forceinline__ f32x4 mfma16(v4u a, v4u b, f32x4 c) {
  return __builtin_amdgcn_mfma_f32_16x16x32_bf16(__builtin_bit_cast(bf16x8_t, a), __builtin_bit_cast(bf16x8_t, b), c, 0, 0, 0);
}
__device__ __forceinline__ v2u tr_read(const unsigned char* p) {
  v4s r = __builtin_amdgcn_ds_read_tr16_b64_v4i16((LDS_AS v4s*)p);
  return __builtin_bit_cast(v2u, r);
}
template <int AUX>
__device__ __forceinline__ void dma16(__amdgpu_buffer_rsrc_t rs, unsigned ldsoff, int voff, int soff) {
  if constexpr (AUX == 2)
    asm volatile("s_nop 4\n\ts_mov_b32 m0, %0\n\ts_nop 0\n\tbuffer_load_dwordx4 %1, %2, %3 offen nt lds" :: "s"(ldsoff), "v"(voff), "s"(rs), "s"(soff) : "m0", "memory");
  else
    asm volatile("s_nop 4\n\ts_mov_b32 m0, %0\n\ts_nop 0\n\tbuffer_load_dwordx4 %1, %2, %3 offen lds" :: "s"(ldsoff), "v"(voff), "s"(rs), "s"(soff) : "m0", "memory");
}
#define DMA16(RS, LDSOFF, VOFF, SOFF, AUX) dma16<AUX>((RS), lds0 + (LDSOFF), (VOFF), (SOFF))
#define WAITV(n) asm volatile("s_waitcnt vmcnt(" #n ")" ::: "memory")
#define RBAR() { asm volatile("s_waitcnt lgkmcnt(0)" ::: "memory"); __builtin_amdgcn_sched_barrier(0); __builtin_amdgcn_s_barrier(); __builtin_amdgcn_sched_barrier(0); }

constexpr int KIMG = 2 * QSLOT;
__device__ __forceinline__ int next_slot(int cur) { return cur == 0 ? 2 * QSLOT : cur - QSLOT; }

__global__ __launch_bounds__(768, 3) void gqa_kernel_a(const unsigned char* __restrict__ x, const unsigned char* __restrict__ kc,
                                                       const unsigned char* __restrict__ vc, const unsigned char* __restrict__ wq,
                                                       const unsigned char* __restrict__ wo, unsigned char* __restrict__ ws) {
  __shared__ __attribute__((aligned(16))) unsigned char smem[SMEM_A];
  STAMP_BEGIN
  TS(0)
  const int bid = blockIdx.x;
  const int xcd = bid & 7, bi = bid >> 3;
  const int type = bi & 1, h = 2 * xcd + ((bi >> 1) & 1), bp = bi >> 2;
  const int gk = h >> 1;
  const int tid = threadIdx.x, lane = tid & 63;
  const int w = __builtin_amdgcn_readfirstlane(tid >> 6);
  const int lg = lane >> 4, lc = lane & 15;
  const int hi = lane >> 5;
  const int q4 = (lane & 15) >> 2, p4 = lane & 3;
  const int bb = (w >> 2) & 1;
  const int cq = w & 3;
  const unsigned lds0 = (unsigned)(size_t)(LDS_AS unsigned char*)smem;

  if (type == 0) {
    if (w >= 8) {
      const int ld = w - 8;
      const __amdgpu_buffer_rsrc_t rsw = __builtin_amdgcn_make_buffer_rsrc((void*)wq, 0, DM * HQ * DH * 2, 0x00020000);
      const __amdgpu_buffer_rsrc_t rsx = __builtin_amdgcn_make_buffer_rsrc((void*)(x + (size_t)bp * (2 * SQ * DM * 2)), 0, 2 * SQ * DM * 2, 0x00020000);
      const __amdgpu_buffer_rsrc_t rsk = __builtin_amdgcn_make_buffer_rsrc((void*)kc, 0, NB * HKV * SK * DH * 2, 0x00020000);
      int wv[4], xv[2];
#pragma unroll
      for (int v = 0; v < 4; ++v) {
        const int sw = (2 * (v & 1) + hi) | ((v >> 1) << 2);
        wv[v] = hi * 8192 + h * 512 + (((lane & 31) ^ (sw << 1)) * 16);
      }
#pragma unroll
      for (int v = 0; v < 2; ++v) xv[v] = (lane >> 3) * 896 + (((lane & 7) ^ ((4 * v + (lane >> 4)) & 7)) * 16);
      int islot = 0;
#pragma unroll 1
      for (int c = -2; c < 5; ++c) {
        if (c >= 0) { WAITV(11); RBAR() }
        int cc = c + 2 + ROT * (bp % 7);
        if (cc >= 7) cc -= 7;
        const unsigned slot = islot;
#pragma unroll
        for (int i = 0; i < 3; ++i) DMA16(rsx, slot + 32768 + (3 * ld + i) * 1024, ((3 * ld + i) & 1) ? xv[1] : xv[0], (3 * ld + i) * 8 * 896 + 128 * cc, AUXX);
#pragma unroll
        for (int i = 0; i < 8; ++i)
          DMA16(rsw, slot + ld * 8192 + i * 1024, wv[(i & 1) | (((i >> 2) & 1) << 1)], (2 * cc * 32 + 16 * ld + 2 * i) * 8192, AUXW);
        islot = next_slot(islot);
      }
      WAITV(11);
      RBAR()
      {
#pragma unroll 1
        for (int ii = 0; ii < 12; ++ii) {
          const int i = 12 * (ld & 1) + ii;
          const int kvo = hi * 512 + (((lane & 31) ^ ((2 * i + hi) & 15)) * 16);
          DMA16(rsk, KIMG + (ld >> 1) * 24576 + i * 1024, kvo, ((2 * bp + (ld >> 1)) * HKV + gk) * (SK * DH * 2) + i * 1024, AUXKV);
        }
      }
      WAITV(12);
      RBAR()
      WAITV(0);
      RBAR()
      RBAR()
    } else {
      f32x4 acc[3][4];
#pragma unroll
      for (int qt = 0; qt < 3; ++qt)
#pragma unroll
        for (int j = 0; j < 4; ++j) acc[qt][j] = f32x4{0.f, 0.f, 0.f, 0.f};
      const int xrd = 32768 + (48 * bb + lc) * 128 + ((lg ^ ((lc >> 1) & 7)) * 16);
      const int srd = q4 | ((lg & 1) << 2);
      const int wrd = (8 * lg + q4) * 512 + (((8 * cq + (p4 >> 1)) ^ (srd << 1)) * 16) + (p4 & 1) * 8;
      int cslot = 0;
      v4u xa[3], xb2[3], wa[4], wb[4];
      auto rd = [&](v4u (&xf)[3], v4u (&wf)[4], const unsigned char* slot, int ss) {
#pragma unroll
        for (int qt = 0; qt < 3; ++qt) xf[qt] = *(const v4u*)(slot + ((xrd ^ (64 * ss)) + qt * 2048));
#pragma unroll
        for (int j = 0; j < 4; ++j) {
          v2u a = tr_read(slot + ss * 16384 + (wrd ^ (32 * j)));
          v2u b = tr_read(slot + ss * 16384 + (wrd ^ (32 * j)) + 2048);
          wf[j] = v4u{a[0], a[1], b[0], b[1]};
        }
      };
      auto mm = [&](const v4u (&xf)[3], const v4u (&wf)[4]) {
#pragma unroll
        for (int j = 0; j < 4; ++j)
#pragma unroll
          for (int qt = 0; qt < 3; ++qt) acc[qt][j] = mfma16(wf[j], xf[qt], acc[qt][j]);
      };
      TS(1)
      RBAR()
      rd(xa, wa, smem, 0);
#pragma unroll
      for (int c = 0; c < 7; ++c) {
        const unsigned char* slot = smem + cslot;
        rd(xb2, wb, slot, 1);
        __builtin_amdgcn_sched_barrier(0);
        mm(xa, wa);
        __builtin_amdgcn_sched_barrier(0);
        cslot = next_slot(cslot);
        RBAR()
        if (c < 6) rd(xa, wa, smem + cslot, 0);
        __builtin_amdgcn_sched_barrier(0);
        mm(xb2, wb);
        __builtin_amdgcn_sched_barrier(0);
      }
      f32x4 sacc[3][3];
      {
        const unsigned char* kl = smem + KIMG + (48 * bb + lc) * 512 + (lg & 1) * 8;
        v4u ka[3][2];
#pragma unroll
        for (int kt = 0; kt < 3; ++kt)
#pragma unroll
          for (int f = 0; f < 2; ++f) {
            v2u a = *(const v2u*)(kl + kt * 8192 + (((8 * cq + 4 * f + (lg >> 1)) ^ lc) * 16));
            v2u b = *(const v2u*)(kl + kt * 8192 + (((8 * cq + 4 * f + 2 + (lg >> 1)) ^ lc) * 16));
            ka[kt][f] = v4u{a[0], a[1], b[0], b[1]};
          }
#pragma unroll
        for (int qt = 0; qt < 3; ++qt) {
#pragma unroll
          for (int kt = 0; kt < 3; ++kt) sacc[kt][qt] = f32x4{0.f, 0.f, 0.f, 0.f};
#pragma unroll
          for (int f = 0; f < 2; ++f) {
            v4u qf;
            qf[0] = pack_bf16(acc[qt][2 * f][0], acc[qt][2 * f][1]);
            qf[1] = pack_bf16(acc[qt][2 * f][2], acc[qt][2 * f][3]);
            qf[2] = pack_bf16(acc[qt][2 * f + 1][0], acc[qt][2 * f + 1][1]);
            qf[3] = pack_bf16(acc[qt][2 * f + 1][2], acc[qt][2 * f + 1][3]);
#pragma unroll
            for (int kt = 0; kt < 3; ++kt) sacc[kt][qt] = mfma16(ka[kt][f], qf, sacc[kt][qt]);
          }
        }
      }
      TS(9)
      unsigned char* sp = smem;
#pragma unroll
      for (int kt = 0; kt < 3; ++kt)
#pragma unroll
        for (int qt = 0; qt < 3; ++qt) *(f32x4*)(sp + ((w * 9 + qt * 3 + kt) * 64 + lane) * 16) = sacc[kt][qt];
      TS(10)
      RBAR()
      TS(11)
      if (w < 6) {
        const int b2 = w >= 3 ? 1 : 0, qt = w - 3 * b2;
        f32x4 s[3];
#pragma unroll
        for (int kt = 0; kt < 3; ++kt) {
          f32x4 a = *(const f32x4*)(sp + (((4 * b2 + 0) * 9 + qt * 3 + kt) * 64 + lane) * 16);
#pragma unroll
          for (int wv = 1; wv < 4; ++wv) a += *(const f32x4*)(sp + (((4 * b2 + wv) * 9 + qt * 3 + kt) * 64 + lane) * 16);
          s[kt] = a;
        }
        float m = -3.0e38f;
#pragma unroll
        for (int kt = 0; kt < 3; ++kt)
#pragma unroll
          for (int r = 0; r < 4; ++r) {
            float t = round_bf16(s[kt][r]) * 0.0625f;
            s[kt][r] = t;
            m = fmaxf(m, t);
          }
        m = fmaxf(m, __shfl_xor(m, 16));
        m = fmaxf(m, __shfl_xor(m, 32));
        float sum = 0.f;
#pragma unroll
        for (int kt = 0; kt < 3; ++kt)
#pragma unroll
          for (int r = 0; r < 4; ++r) {
            float e = __expf(s[kt][r] - m);
            s[kt][r] = e;
            sum += e;
          }
        sum += __shfl_xor(sum, 16);
        sum += __shfl_xor(sum, 32);
        const float inv = 1.0f / sum;
        v4u p1;
        v2u p2;
        p1[0] = pack_bf16(s[0][0] * inv, s[0][1] * inv);
        p1[1] = pack_bf16(s[0][2] * inv, s[0][3] * inv);
        p1[2] = pack_bf16(s[1][0] * inv, s[1][1] * inv);
        p1[3] = pack_bf16(s[1][2] * inv, s[1][3] * inv);
        p2[0] = pack_bf16(s[2][0] * inv, s[2][1] * inv);
        p2[1] = pack_bf16(s[2][2] * inv, s[2][3] * inv);
        unsigned char* pb = ws + P_OFF + (size_t)((2 * bp + b2) * 16 + h) * P_BLK;
        *(v4u*)(pb + qt * 1024 + lane * 16) = p1;
        *(v2u*)(pb + P_R2 + qt * 512 + lane * 8) = p2;
      }
    }
  } else {
    if (w >= 8) {
      const int ld = w - 8;
      const __amdgpu_buffer_rsrc_t rs = __builtin_amdgcn_make_buffer_rsrc((void*)wo, 0, DM * HQ * DH * 2, 0x00020000);
      const __amdgpu_buffer_rsrc_t rsv = __builtin_amdgcn_make_buffer_rsrc((void*)vc, 0, NB * HKV * SK * DH * 2, 0x00020000);
      int vv[2];
#pragma unroll
      for (int v = 0; v < 2; ++v) vv[v] = (lane >> 3) * 512 + (((lane & 7) ^ ((4 * v + (lane >> 4)) & 7)) * 16);
      int wo_voff[2][7];
#pragma unroll
      for (int a = 0; a < 2; ++a)
#pragma unroll
        for (int r = 0; r < 7; ++r) {
          const int P = 1024 * r + 16 * lane;
          const int row = P / 896;
          const int phys = (P - 896 * row) >> 4;
          const int sp_ = ((row >> 1) & 1) | (a << 1);
          wo_voff[a][r] = row * 896 + ((phys ^ (sp_ << 1)) * 16);
        }
#pragma unroll 1
      for (int c = 0; c < 4; ++c) {
        if (c >= 2) { if (c == 2) { WAITV(17); RBAR() } WAITV(0); RBAR() }
        const int kp = (c + ROT * bp) & 3;
#pragma unroll
        for (int i = 0; i < 3; ++i)
          DMA16(rsv, c * 12288 + (3 * ld + i) * 1024, ((3 * ld + i) & 1) ? vv[1] : vv[0],
                ((2 * bp + (ld >> 1)) * HKV + gk) * (SK * DH * 2) + (3 * (ld & 1) + i) * 8 * 512 + 128 * kp, AUXKV);
        const unsigned slot = VIMG + (c & 1) * VSLOT + ld * 14336;
#pragma unroll
        for (int a = 0; a < 2; ++a)
#pragma unroll
          for (int r = 0; r < 7; ++r) DMA16(rs, slot + (7 * a + r) * 1024, wo_voff[a][r], (h * 256 + 64 * kp + 16 * ld + 8 * a) * 896, AUXW);
      }
      WAITV(0);
      RBAR()
    } else {
      const int cw = cq;
      f32x4 acc[3][7];
#pragma unroll
      for (int kt = 0; kt < 3; ++kt)
#pragma unroll
        for (int ct = 0; ct < 7; ++ct) acc[kt][ct] = f32x4{0.f, 0.f, 0.f, 0.f};
      const int vrd = (48 * bb + lc) * 128 + ((lg ^ ((lc >> 1) & 7)) * 16);
      const int sp2 = ((q4 >> 1) & 1) | ((lg & 1) << 1);
      const int wrow = (8 * lg + q4) * 896 + (p4 & 1) * 8;
      v4u va[3], vb[3], wa[4], wb[3];
      auto rdv = [&](v4u (&vf)[3], int c, int ss) {
#pragma unroll
        for (int kt = 0; kt < 3; ++kt) vf[kt] = *(const v4u*)(smem + c * 12288 + ((vrd ^ (64 * ss)) + kt * 2048));
      };
      int wbase[4];
#pragma unroll
      for (int i = 0; i < 4; ++i) wbase[i] = VIMG + wrow + (((14 * cw + 2 * i + (p4 >> 1)) ^ (sp2 << 1)) * 16);
      auto rdw = [&](v4u* wf, int c, int ss, int ct0, int n) {
#pragma unroll
        for (int i = 0; i < n; ++i) {
          const unsigned char* p = smem + wbase[i] + ((c & 1) * VSLOT + ss * 28672 + (ct0 ? 128 : 0));
          v2u a = tr_read(p);
          v2u b = tr_read(p + 3584);
          wf[i] = v4u{a[0], a[1], b[0], b[1]};
        }
      };
      auto mm = [&](const v4u (&vf)[3], const v4u* wf, int ct0, int n) {
#pragma unroll
        for (int i = 0; i < n; ++i)
#pragma unroll
          for (int kt = 0; kt < 3; ++kt) acc[kt][ct0 + i] = mfma16(vf[kt], wf[i], acc[kt][ct0 + i]);
      };
#define SB __builtin_amdgcn_sched_barrier(0);
      TS(1)
      RBAR()
      rdv(va, 0, 0);
      rdw(wa, 0, 0, 0, 4);
#pragma unroll
      for (int c = 0; c < 4; ++c) {
        rdw(wb, c, 0, 4, 3); SB
        mm(va, wa, 0, 4); SB
        rdv(vb, c, 1); rdw(wa, c, 1, 0, 4); SB
        mm(va, wb, 4, 3); SB
        rdw(wb, c, 1, 4, 3); SB
        mm(vb, wa, 0, 4); SB
        if (c < 3) {
          RBAR()
          rdv(va, c + 1, 0);
          rdw(wa, c + 1, 0, 0, 4);
        }
        SB
        mm(vb, wb, 4, 3); SB
      }
      const __amdgpu_buffer_rsrc_t rso = __builtin_amdgcn_make_buffer_rsrc((void*)ws, 0, 256 * VW_BLK, 0x00020000);
      {
        const int ob = ((2 * bp + bb) * 16 + h) * VW_BLK;
        const int o1 = ob + 7 * cw * 1024 + lane * 16;
        const int o2 = ob + VW_R2 + 7 * cw * 512 + lane * 8;
#pragma unroll
        for (int ct = 0; ct < 7; ++ct) {
          v4u o;
          o[0] = pack_bf16(acc[0][ct][0], acc[0][ct][1]);
          o[1] = pack_bf16(acc[0][ct][2], acc[0][ct][3]);
          o[2] = pack_bf16(acc[1][ct][0], acc[1][ct][1]);
          o[3] = pack_bf16(acc[1][ct][2], acc[1][ct][3]);
          __builtin_amdgcn_raw_buffer_store_b128(o, rso, o1 + ct * 1024, 0, AUXST);
          v2u o3;
          o3[0] = pack_bf16(acc[2][ct][0], acc[2][ct][1]);
          o3[1] = pack_bf16(acc[2][ct][2], acc[2][ct][3]);
          __builtin_amdgcn_raw_buffer_store_b64(o3, rso, o2 + ct * 512, 0, AUXST);
        }
      }
      TS(10)
    }
  }
  TS(12)
  TSDUMP(ws)
  STAMP_END(0, ws)
}

__global__ __launch_bounds__(512) void gqa_kernel_b(const unsigned char* __restrict__ ws, unsigned char* __restrict__ out) {
  __shared__ __attribute__((aligned(16))) float red[8 * 48 * 36];
  STAMP_BEGIN
  const int bid = blockIdx.x;
  const int xcd = bid & 7, bi = bid >> 3;
  const int b = xcd + 8 * (bi / 14), m = bi % 14;
  const int tid = threadIdx.x, lane = tid & 63;
  const int w = __builtin_amdgcn_readfirstlane(tid >> 6);
  const int lg = lane >> 4, lc = lane & 15;

  v4u p1[2][3], v1[2][2];
  v2u p2[2][3], v2[2][2];
#pragma unroll
  for (int hh = 0; hh < 2; ++hh) {
    const int h = 2 * w + hh;
    const unsigned char* pb = ws + P_OFF + (size_t)(b * 16 + h) * P_BLK;
    const unsigned char* vb = ws + (size_t)(b * 16 + h) * VW_BLK;
#pragma unroll
    for (int ct = 0; ct < 2; ++ct) {
      v1[hh][ct] = *(const v4u*)(vb + (2 * m + ct) * 1024 + lane * 16);
      v2[hh][ct] = *(const v2u*)(vb + VW_R2 + (2 * m + ct) * 512 + lane * 8);
    }
#pragma unroll
    for (int qt = 0; qt < 3; ++qt) {
      p1[hh][qt] = *(const v4u*)(pb + qt * 1024 + lane * 16);
      p2[hh][qt] = *(const v2u*)(pb + P_R2 + qt * 512 + lane * 8);
    }
  }
#pragma unroll
  for (int qt = 0; qt < 3; ++qt)
#pragma unroll
    for (int ct = 0; ct < 2; ++ct) {
      f32x4 a = f32x4{0.f, 0.f, 0.f, 0.f};
      a = mfma16(p1[0][qt], v1[0][ct], a);
      a = mfma16(p1[1][qt], v1[1][ct], a);
      v4u pa = {p2[0][qt][0], p2[0][qt][1], p2[1][qt][0], p2[1][qt][1]};
      v4u va = {v2[0][ct][0], v2[0][ct][1], v2[1][ct][0], v2[1][ct][1]};
      a = mfma16(pa, va, a);
#pragma unroll
      for (int r = 0; r < 4; ++r) red[(w * 48 + 16 * qt + 4 * lg + r) * 36 + 16 * ct + lc] = a[r];
    }
  __syncthreads();
  if (tid < 384) {
    const int row = tid >> 3, c4 = tid & 7;
    f32x4 s = *(const f32x4*)(red + row * 36 + 4 * c4);
#pragma unroll
    for (int wv = 1; wv < 8; ++wv) s += *(const f32x4*)(red + (wv * 48 + row) * 36 + 4 * c4);
    v2u o = {pack_bf16(s[0], s[1]), pack_bf16(s[2], s[3])};
    *(v2u*)(out + (((size_t)b * SQ + row) * DM + 32 * m + 4 * c4) * 2) = o;
  }
  STAMP_END(1, ws)
}

extern "C" void kernel_launch(void* const* d_in, const int* in_sizes, int n_in, void* d_out, int out_size, void* d_ws,
                              size_t ws_size, hipStream_t stream) {
  const unsigned char* x = (const unsigned char*)d_in[0];
  const unsigned char* kc = (const unsigned char*)d_in[1];
  const unsigned char* vc = (const unsigned char*)d_in[2];
  const unsigned char* wq = (const unsigned char*)d_in[3];
  const unsigned char* wo = (const unsigned char*)d_in[4];
  unsigned char* ws = (unsigned char*)d_ws;
  gqa_kernel_a<<<256, 768, 0, stream>>>(x, kc, vc, wq, wo, ws);
  gqa_kernel_b<<<224, 512, 0, stream>>>(ws, (unsigned char*)d_out);
}
```

```cpp
#include <hip/hip_runtime.h>
#include <hip/hip_bf16.h>
#include <stdint.h>

typedef unsigned v4u __attribute__((ext_vector_type(4)));
typedef unsigned v2u __attribute__((ext_vector_type(2)));
typedef short v4s __attribute__((ext_vector_type(4)));
typedef float f32x4 __attribute__((ext_vector_type(4)));
typedef float f32x2 __attribute__((ext_vector_type(2)));
typedef __bf16 bf16x8_t __attribute__((ext_vector_type(8)));
typedef __bf16 bf16x2_t __attribute__((ext_vector_type(2)));
#define LDS_AS __attribute__((address_space(3)))

constexpr int NB = 16, SQ = 48, DM = 448, HQ = 16, HKV = 8, SK = 48, DH = 256;
constexpr int SMEM_A = 163840;
constexpr int QSLOT = 45056;
constexpr int VSLOT = 57344;
constexpr int VIMG = 49152;
constexpr int VW_BLK = 43008;
constexpr int VW_R2 = 28672;
constexpr int P_BLK = 4608;
constexpr int P_R2 = 3072;
constexpr size_t P_OFF = (size_t)256 * VW_BLK;

#define TS(i)
#define TSDUMP(WSP)
#define STAMP_BEGIN
#define STAMP_END(K, WSP)

#define AUXW 0
#define AUXX 0
#define AUXKV 0
#define AUXT 2
#define TOUCH 1
#define ROT 0
#define AUXST 16

__device__ __forceinline__ unsigned pack_bf16(float a, float b) {
  f32x2 v = {a, b};
  bf16x2_t r = __builtin_convertvector(v, bf16x2_t);
  return __builtin_bit_cast(unsigned, r);
}
__device__ __forceinline__ float round_bf16(float a) {
  __bf16 r = (__bf16)a;
  return (float)r;
}
__device__ __forceinline__ f32x4 mfma16(v4u a, v4u b, f32x4 c) {
  return __builtin_amdgcn_mfma_f32_16x16x32_bf16(__builtin_bit_cast(bf16x8_t, a), __builtin_bit_cast(bf16x8_t, b), c, 0, 0, 0);
}
__device__ __forceinline__ void swap16(float& a, float& b) { asm volatile("s_nop 1\n\tv_permlane16_swap_b32 %0, %1\n\ts_nop 1" : "+v"(a), "+v"(b)); }
__device__ __forceinline__ void swap32(float& a, float& b) { asm volatile("s_nop 1\n\tv_permlane32_swap_b32 %0, %1\n\ts_nop 1" : "+v"(a), "+v"(b)); }
__device__ __forceinline__ float xlane_max(float v) {
  float a = v, b = v;
  swap16(a, b);
  a = fmaxf(a, b); b = a;
  swap32(a, b);
  return fmaxf(a, b);
}
__device__ __forceinline__ float xlane_sum(float v) {
  float a = v, b = v;
  swap16(a, b);
  a = a + b; b = a;
  swap32(a, b);
  return a + b;
}
__device__ __forceinline__ v2u tr_read(const unsigned char* p) {
  v4s r = __builtin_amdgcn_ds_read_tr16_b64_v4i16((LDS_AS v4s*)p);
  return __builtin_bit_cast(v2u, r);
}
template <int AUX>
__device__ __forceinline__ void dma16(__amdgpu_buffer_rsrc_t rs, unsigned ldsoff, int voff, int soff) {
  if constexpr (AUX == 2)
    asm volatile("s_nop 4\n\ts_mov_b32 m0, %0\n\ts_nop 0\n\tbuffer_load_dwordx4 %1, %2, %3 offen nt lds" :: "s"(ldsoff), "v"(voff), "s"(rs), "s"(soff) : "m0", "memory");
  else
    asm volatile("s_nop 4\n\ts_mov_b32 m0, %0\n\ts_nop 0\n\tbuffer_load_dwordx4 %1, %2, %3 offen lds" :: "s"(ldsoff), "v"(voff), "s"(rs), "s"(soff) : "m0", "memory");
}
#define DMA16(RS, LDSOFF, VOFF, SOFF, AUX) dma16<AUX>((RS), lds0 + (LDSOFF), (VOFF), (SOFF))
#define WAITV(n) asm volatile("s_waitcnt vmcnt(" #n ")" ::: "memory")
#define RBAR() { asm volatile("s_waitcnt lgkmcnt(0)" ::: "memory"); __builtin_amdgcn_sched_barrier(0); __builtin_amdgcn_s_barrier(); __builtin_amdgcn_sched_barrier(0); }

constexpr int KIMG = 2 * QSLOT;
__device__ __forceinline__ int next_slot(int cur) { return cur == 0 ? 2 * QSLOT : cur - QSLOT; }

__global__ __launch_bounds__(768, 3) void gqa_kernel_a(const unsigned char* __restrict__ x, const unsigned char* __restrict__ kc,
                                                       const unsigned char* __restrict__ vc, const unsigned char* __restrict__ wq,
                                                       const unsigned char* __restrict__ wo, unsigned char* __restrict__ ws) {
  __shared__ __attribute__((aligned(16))) unsigned char smem[SMEM_A];
  STAMP_BEGIN
  TS(0)
  const int bid = blockIdx.x;
  const int xcd = bid & 7, bi = bid >> 3;
  const int type = bi & 1, h = 2 * xcd + ((bi >> 1) & 1), bp = bi >> 2;
  const int gk = h >> 1;
  const int tid = threadIdx.x, lane = tid & 63;
  const int w = __builtin_amdgcn_readfirstlane(tid >> 6);
  const int lg = lane >> 4, lc = lane & 15;
  const int hi = lane >> 5;
  const int q4 = (lane & 15) >> 2, p4 = lane & 3;
  const int bb = (w >> 2) & 1;
  const int cq = w & 3;
  const unsigned lds0 = (unsigned)(size_t)(LDS_AS unsigned char*)smem;

  if (type == 0) {
    if (w >= 8) {
      const int ld = w - 8;
      const __amdgpu_buffer_rsrc_t rsw = __builtin_amdgcn_make_buffer_rsrc((void*)wq, 0, DM * HQ * DH * 2, 0x00020000);
      const __amdgpu_buffer_rsrc_t rsx = __builtin_amdgcn_make_buffer_rsrc((void*)(x + (size_t)bp * (2 * SQ * DM * 2)), 0, 2 * SQ * DM * 2, 0x00020000);
      const __amdgpu_buffer_rsrc_t rsk = __builtin_amdgcn_make_buffer_rsrc((void*)kc, 0, NB * HKV * SK * DH * 2, 0x00020000);
      int wv[4], xv[2];
#pragma unroll
      for (int v = 0; v < 4; ++v) {
        const int sw = (2 * (v & 1) + hi) | ((v >> 1) << 2);
        wv[v] = hi * 8192 + h * 512 + (((lane & 31) ^ (sw << 1)) * 16);
      }
#pragma unroll
      for (int v = 0; v < 2; ++v) xv[v] = (lane >> 3) * 896 + (((lane & 7) ^ ((4 * ((ld + v) & 1) + (lane >> 4)) & 7)) * 16);
      auto issue = [&](int cc, unsigned slot, int lo, int hi_) {
#pragma unroll
        for (int i = 0; i < 3; ++i) if (i >= lo && i < hi_) DMA16(rsx, slot + 32768 + (3 * ld + i) * 1024, xv[i & 1], (3 * ld + i) * 8 * 896 + 128 * cc, AUXX);
#pragma unroll
        for (int i = 0; i < 8; ++i) if (i + 3 >= lo && i + 3 < hi_)
          DMA16(rsw, slot + ld * 8192 + i * 1024, wv[(i & 1) | (((i >> 2) & 1) << 1)], (2 * cc * 32 + 16 * ld + 2 * i) * 8192, AUXW);
      };
      issue(0, 0, 0, 11);
      issue(1, 2 * QSLOT, 0, 6);
      WAITV(6); RBAR()
      issue(1, 2 * QSLOT, 6, 11);
      issue(2, QSLOT, 0, 11);
      int islot = 0;
#pragma unroll 1
      for (int c = 1; c < 5; ++c) {
        WAITV(11); RBAR()
        issue(c + 2, islot, 0, 11);
        islot = next_slot(islot);
      }
      WAITV(11);
      RBAR()
      {
#pragma unroll 1
        for (int ii = 0; ii < 12; ++ii) {
          const int i = 12 * (ld & 1) + ii;
          const int kvo = hi * 512 + (((lane & 31) ^ ((2 * i + hi) & 15)) * 16);
          DMA16(rsk, KIMG + (ld >> 1) * 24576 + i * 1024, kvo, ((2 * bp + (ld >> 1)) * HKV + gk) * (SK * DH * 2) + i * 1024, AUXKV);
        }
      }
      WAITV(12);
      RBAR()
      WAITV(0);
      RBAR()
      RBAR()
    } else {
      f32x4 acc[3][4];
#pragma unroll
      for (int qt = 0; qt < 3; ++qt)
#pragma unroll
        for (int j = 0; j < 4; ++j) acc[qt][j] = f32x4{0.f, 0.f, 0.f, 0.f};
      const int xrd = 32768 + (48 * bb + lc) * 128 + ((lg ^ ((lc >> 1) & 7)) * 16);
      const int srd = q4 | ((lg & 1) << 2);
      const int wrd = (8 * lg + q4) * 512 + (((8 * cq + (p4 >> 1)) ^ (srd << 1)) * 16) + (p4 & 1) * 8;
      int cslot = 0;
#define ILQ(n) _Pragma("unroll") for (int i_ = 0; i_ < (n); ++i_) { __builtin_amdgcn_sched_group_barrier(0x8, 1, 0); __builtin_amdgcn_sched_group_barrier(0x100, 1, 0); }
      v4u xa[3], xb2[3], wa[4], wb[4], ka[3][2];
      auto rd = [&](v4u (&xf)[3], v4u (&wf)[4], const unsigned char* slot, int ss) {
#pragma unroll
        for (int qt = 0; qt < 3; ++qt) xf[qt] = *(const v4u*)(slot + ((xrd ^ (64 * ss)) + qt * 2048));
#pragma unroll
        for (int j = 0; j < 4; ++j) {
          v2u a = tr_read(slot + ss * 16384 + (wrd ^ (32 * j)));
          v2u b = tr_read(slot + ss * 16384 + (wrd ^ (32 * j)) + 2048);
          wf[j] = v4u{a[0], a[1], b[0], b[1]};
        }
      };
      auto mm = [&](const v4u (&xf)[3], const v4u (&wf)[4]) {
#pragma unroll
        for (int j = 0; j < 4; ++j)
#pragma unroll
          for (int qt = 0; qt < 3; ++qt) acc[qt][j] = mfma16(wf[j], xf[qt], acc[qt][j]);
      };
      TS(1)
      RBAR()
      rd(xa, wa, smem, 0);
#pragma unroll
      for (int c = 0; c < 7; ++c) {
        const unsigned char* slot = smem + cslot;
        rd(xb2, wb, slot, 1);
        mm(xa, wa);
        ILQ(11)
        __builtin_amdgcn_sched_barrier(0);
        cslot = next_slot(cslot);
        RBAR()
        if (c < 6) rd(xa, wa, smem + cslot, 0);
        else {
          const unsigned char* kl = smem + KIMG + (48 * bb + lc) * 512 + (lg & 1) * 8;
#pragma unroll
          for (int kt = 0; kt < 3; ++kt)
#pragma unroll
            for (int f = 0; f < 2; ++f) {
              v2u a = *(const v2u*)(kl + kt * 8192 + (((8 * cq + 4 * f + (lg >> 1)) ^ lc) * 16));
              v2u b = *(const v2u*)(kl + kt * 8192 + (((8 * cq + 4 * f + 2 + (lg >> 1)) ^ lc) * 16));
              ka[kt][f] = v4u{a[0], a[1], b[0], b[1]};
            }
        }
        mm(xb2, wb);
        ILQ(11)
        __builtin_amdgcn_sched_barrier(0);
      }
      f32x4 sacc[3][3];
      {
#pragma unroll
        for (int qt = 0; qt < 3; ++qt) {
#pragma unroll
          for (int kt = 0; kt < 3; ++kt) sacc[kt][qt] = f32x4{0.f, 0.f, 0.f, 0.f};
#pragma unroll
          for (int f = 0; f < 2; ++f) {
            v4u qf;
            qf[0] = pack_bf16(acc[qt][2 * f][0], acc[qt][2 * f][1]);
            qf[1] = pack_bf16(acc[qt][2 * f][2], acc[qt][2 * f][3]);
            qf[2] = pack_bf16(acc[qt][2 * f + 1][0], acc[qt][2 * f + 1][1]);
            qf[3] = pack_bf16(acc[qt][2 * f + 1][2], acc[qt][2 * f + 1][3]);
#pragma unroll
            for (int kt = 0; kt < 3; ++kt) sacc[kt][qt] = mfma16(ka[kt][f], qf, sacc[kt][qt]);
          }
        }
      }
      TS(9)
      unsigned char* sp = smem;
#pragma unroll
      for (int kt = 0; kt < 3; ++kt)
#pragma unroll
        for (int qt = 0; qt < 3; ++qt) *(f32x4*)(sp + ((w * 9 + qt * 3 + kt) * 64 + lane) * 16) = sacc[kt][qt];
      TS(10)
      RBAR()
      TS(11)
      if (w < 6) {
        const int b2 = w >= 3 ? 1 : 0, qt = w - 3 * b2;
        f32x4 pr[4][3];
        const unsigned char* spl = sp + ((36 * b2 + qt * 3) * 64 + lane) * 16;
#pragma unroll
        for (int wv = 0; wv < 4; ++wv)
#pragma unroll
          for (int kt = 0; kt < 3; ++kt) pr[wv][kt] = *(const f32x4*)(spl + (wv * 9 + kt) * 1024);
        __builtin_amdgcn_sched_barrier(0);
        float sv[12];
#pragma unroll
        for (int kt = 0; kt < 3; ++kt) {
          const f32x4 a = (pr[0][kt] + pr[1][kt]) + (pr[2][kt] + pr[3][kt]);
          const unsigned u0 = pack_bf16(a[0], a[1]), u1 = pack_bf16(a[2], a[3]);
          sv[4 * kt + 0] = __builtin_bit_cast(float, u0 << 16);
          sv[4 * kt + 1] = __builtin_bit_cast(float, u0 & 0xffff0000u);
          sv[4 * kt + 2] = __builtin_bit_cast(float, u1 << 16);
          sv[4 * kt + 3] = __builtin_bit_cast(float, u1 & 0xffff0000u);
        }
        float m = fmaxf(fmaxf(fmaxf(sv[0], sv[1]), fmaxf(sv[2], sv[3])), fmaxf(fmaxf(sv[4], sv[5]), fmaxf(sv[6], sv[7])));
        m = fmaxf(m, fmaxf(fmaxf(sv[8], sv[9]), fmaxf(sv[10], sv[11])));
        m = xlane_max(m);
        const float cs = 0.0625f * 1.44269504088896340736f;
        const float mc = -m * cs;
        float sum = 0.f;
#pragma unroll
        for (int i = 0; i < 12; ++i) {
          sv[i] = __builtin_amdgcn_exp2f(__builtin_fmaf(sv[i], cs, mc));
          sum += sv[i];
        }
        sum = xlane_sum(sum);
        const float inv = __builtin_amdgcn_rcpf(sum);
        v4u p1;
        v2u p2;
        p1[0] = pack_bf16(sv[0] * inv, sv[1] * inv);
        p1[1] = pack_bf16(sv[2] * inv, sv[3] * inv);
        p1[2] = pack_bf16(sv[4] * inv, sv[5] * inv);
        p1[3] = pack_bf16(sv[6] * inv, sv[7] * inv);
        p2[0] = pack_bf16(sv[8] * inv, sv[9] * inv);
        p2[1] = pack_bf16(sv[10] * inv, sv[11] * inv);
        unsigned char* pb = ws + P_OFF + (size_t)((2 * bp + b2) * 16 + h) * P_BLK;
        *(v4u*)(pb + qt * 1024 + lane * 16) = p1;
        *(v2u*)(pb + P_R2 + qt * 512 + lane * 8) = p2;
      }
    }
  } else {
    if (w >= 8) {
      const int ld = w - 8;
      const __amdgpu_buffer_rsrc_t rs = __builtin_amdgcn_make_buffer_rsrc((void*)wo, 0, DM * HQ * DH * 2, 0x00020000);
      const __amdgpu_buffer_rsrc_t rsv = __builtin_amdgcn_make_buffer_rsrc((void*)vc, 0, NB * HKV * SK * DH * 2, 0x00020000);
      int vv[2];
#pragma unroll
      for (int v = 0; v < 2; ++v) vv[v] = (lane >> 3) * 512 + (((lane & 7) ^ ((4 * ((ld + v) & 1) + (lane >> 4)) & 7)) * 16);
      int wo_voff[7];
#pragma unroll
      for (int r = 0; r < 7; ++r) {
        const int P = 1024 * r + 16 * lane;
        const int row = P / 896;
        const int phys = (P - 896 * row) >> 4;
        const int sp_ = ((row >> 1) & 1) | ((ld & 1) << 1);
        wo_voff[r] = row * 896 + ((phys ^ (sp_ << 1)) * 16);
      }
      auto issue = [&](int st) {
        if ((st & 1) == 0) {
#pragma unroll
          for (int i = 0; i < 3; ++i)
            DMA16(rsv, (st >> 1) * 12288 + (3 * ld + i) * 1024, vv[i & 1],
                  ((2 * bp + (ld >> 1)) * HKV + gk) * (SK * DH * 2) + (3 * (ld & 1) + i) * 8 * 512 + 64 * st, AUXKV);
        }
        const unsigned slot = VIMG + (st & 3) * 28672 + ld * 7168;
#pragma unroll
        for (int r = 0; r < 7; ++r) DMA16(rs, slot + r * 1024, wo_voff[r], (h * 256 + 32 * st + 8 * ld) * 896, AUXW);
      };
      issue(0); issue(1);
      WAITV(7);  RBAR() issue(2); issue(3);
      WAITV(17); RBAR() issue(4);
      WAITV(17); RBAR() issue(5);
      WAITV(17); RBAR() issue(6);
      WAITV(17); RBAR() issue(7);
      WAITV(17); RBAR()
      WAITV(7);  RBAR()
      WAITV(0);  RBAR()
    } else {
      const int cw = cq;
      f32x4 acc[3][7];
#pragma unroll
      for (int kt = 0; kt < 3; ++kt)
#pragma unroll
        for (int ct = 0; ct < 7; ++ct) acc[kt][ct] = f32x4{0.f, 0.f, 0.f, 0.f};
      const int vrd = (48 * bb + lc) * 128 + ((lg ^ ((lc >> 1) & 7)) * 16);
      const int sp2 = ((q4 >> 1) & 1) | ((lg & 1) << 1);
      const int wrow = (8 * lg + q4) * 896 + (p4 & 1) * 8;
      v4u va[3], vb[3], wa[4], wb[3];
      auto rdv = [&](v4u (&vf)[3], int c, int ss) {
#pragma unroll
        for (int kt = 0; kt < 3; ++kt) vf[kt] = *(const v4u*)(smem + c * 12288 + ((vrd ^ (64 * ss)) + kt * 2048));
      };
      int wbase[4];
#pragma unroll
      for (int i = 0; i < 4; ++i) wbase[i] = VIMG + wrow + (((14 * cw + 2 * i + (p4 >> 1)) ^ (sp2 << 1)) * 16);
      auto rdw = [&](v4u* wf, int st, int ct0, int n) {
#pragma unroll
        for (int i = 0; i < n; ++i) {
          const unsigned char* p = smem + wbase[i] + ((st & 3) * 28672 + (ct0 ? 128 : 0));
          v2u a = tr_read(p);
          v2u b = tr_read(p + 3584);
          wf[i] = v4u{a[0], a[1], b[0], b[1]};
        }
      };
      auto mm = [&](const v4u (&vf)[3], const v4u* wf, int ct0, int n) {
#pragma unroll
        for (int i = 0; i < n; ++i)
#pragma unroll
          for (int kt = 0; kt < 3; ++kt) acc[kt][ct0 + i] = mfma16(vf[kt], wf[i], acc[kt][ct0 + i]);
      };
#define SB __builtin_amdgcn_sched_barrier(0);
#define IL(n) _Pragma("unroll") for (int i_ = 0; i_ < (n); ++i_) { __builtin_amdgcn_sched_group_barrier(0x8, 1, 0); __builtin_amdgcn_sched_group_barrier(0x100, 1, 0); }
#define IL2(n) _Pragma("unroll") for (int i_ = 0; i_ < (n); ++i_) { __builtin_amdgcn_sched_group_barrier(0x8, 1, 0); __builtin_amdgcn_sched_group_barrier(0x100, 2, 0); }
      TS(1)
      RBAR()
      rdv(va, 0, 0);
      rdw(wa, 0, 0, 4);
#pragma unroll
      for (int c = 0; c < 4; ++c) {
        rdw(wb, 2 * c, 4, 3);
        mm(va, wa, 0, 4); IL(6) SB
        RBAR()
        rdv(vb, c, 1); rdw(wa, 2 * c + 1, 0, 4);
        mm(va, wb, 4, 3); IL2(6) SB
        rdw(wb, 2 * c + 1, 4, 3);
        mm(vb, wa, 0, 4); IL(6) SB
        if (c < 3) {
          RBAR()
          rdv(va, c + 1, 0);
          rdw(wa, 2 * c + 2, 0, 4);
          mm(vb, wb, 4, 3); IL2(6) SB
        } else {
          mm(vb, wb, 4, 3); SB
        }
      }
      const __amdgpu_buffer_rsrc_t rso = __builtin_amdgcn_make_buffer_rsrc((void*)ws, 0, 256 * VW_BLK, 0x00020000);
      {
        const int ob = ((2 * bp + bb) * 16 + h) * VW_BLK;
        const int o1 = ob + 7 * cw * 1024 + lane * 16;
        const int o2 = ob + VW_R2 + 7 * cw * 512 + lane * 8;
#pragma unroll
        for (int ct = 0; ct < 7; ++ct) {
          v4u o;
          o[0] = pack_bf16(acc[0][ct][0], acc[0][ct][1]);
          o[1] = pack_bf16(acc[0][ct][2], acc[0][ct][3]);
          o[2] = pack_bf16(acc[1][ct][0], acc[1][ct][1]);
          o[3] = pack_bf16(acc[1][ct][2], acc[1][ct][3]);
          __builtin_amdgcn_raw_buffer_store_b128(o, rso, o1 + ct * 1024, 0, AUXST);
          v2u o3;
          o3[0] = pack_bf16(acc[2][ct][0], acc[2][ct][1]);
          o3[1] = pack_bf16(acc[2][ct][2], acc[2][ct][3]);
          __builtin_amdgcn_raw_buffer_store_b64(o3, rso, o2 + ct * 512, 0, AUXST);
        }
      }
      TS(10)
    }
  }
  TS(12)
  TSDUMP(ws)
  STAMP_END(0, ws)
}

__global__ __launch_bounds__(512) void gqa_kernel_b(const unsigned char* __restrict__ ws, unsigned char* __restrict__ out) {
  __shared__ __attribute__((aligned(16))) float red[8 * 48 * 36];
  STAMP_BEGIN
  const int bid = blockIdx.x;
  const int xcd = bid & 7, bi = bid >> 3;
  const int b = xcd + 8 * (bi / 14), m = bi % 14;
  const int tid = threadIdx.x, lane = tid & 63;
  const int w = __builtin_amdgcn_readfirstlane(tid >> 6);
  const int lg = lane >> 4, lc = lane & 15;

  v4u p1[2][3], v1[2][2];
  v2u p2[2][3], v2[2][2];
#pragma unroll
  for (int hh = 0; hh < 2; ++hh) {
    const int h = 2 * w + hh;
    const unsigned char* pb = ws + P_OFF + (size_t)(b * 16 + h) * P_BLK;
    const unsigned char* vb = ws + (size_t)(b * 16 + h) * VW_BLK;
#pragma unroll
    for (int ct = 0; ct < 2; ++ct) {
      v1[hh][ct] = *(const v4u*)(vb + (2 * m + ct) * 1024 + lane * 16);
      v2[hh][ct] = *(const v2u*)(vb + VW_R2 + (2 * m + ct) * 512 + lane * 8);
    }
#pragma unroll
    for (int qt = 0; qt < 3; ++qt) {
      p1[hh][qt] = *(const v4u*)(pb + qt * 1024 + lane * 16);
      p2[hh][qt] = *(const v2u*)(pb + P_R2 + qt * 512 + lane * 8);
    }
  }
#pragma unroll
  for (int qt = 0; qt < 3; ++qt)
#pragma unroll
    for (int ct = 0; ct < 2; ++ct) {
      f32x4 a = f32x4{0.f, 0.f, 0.f, 0.f};
      a = mfma16(p1[0][qt], v1[0][ct], a);
      a = mfma16(p1[1][qt], v1[1][ct], a);
      v4u pa = {p2[0][qt][0], p2[0][qt][1], p2[1][qt][0], p2[1][qt][1]};
      v4u va = {v2[0][ct][0], v2[0][ct][1], v2[1][ct][0], v2[1][ct][1]};
      a = mfma16(pa, va, a);
#pragma unroll
      for (int r = 0; r < 4; ++r) red[(w * 48 + 16 * qt + 4 * lg + r) * 36 + 16 * ct + lc] = a[r];
    }
  __syncthreads();
  if (tid < 384) {
    const int row = tid >> 3, c4 = tid & 7;
    f32x4 s = *(const f32x4*)(red + row * 36 + 4 * c4);
#pragma unroll
    for (int wv = 1; wv < 8; ++wv) s += *(const f32x4*)(red + (wv * 48 + row) * 36 + 4 * c4);
    v2u o = {pack_bf16(s[0], s[1]), pack_bf16(s[2], s[3])};
    *(v2u*)(out + (((size_t)b * SQ + row) * DM + 32 * m + 4 * c4) * 2) = o;
  }
  STAMP_END(1, ws)
}

extern "C" void kernel_launch(void* const* d_in, const int* in_sizes, int n_in, void* d_out, int out_size, void* d_ws,
                              size_t ws_size, hipStream_t stream) {
  const unsigned char* x = (const unsigned char*)d_in[0];
  const unsigned char* kc = (const unsigned char*)d_in[1];
  const unsigned char* vc = (const unsigned char*)d_in[2];
  const unsigned char* wq = (const unsigned char*)d_in[3];
  const unsigned char* wo = (const unsigned char*)d_in[4];
  unsigned char* ws = (unsigned char*)d_ws + (((ws_size - (size_t)(16u << 20))) & ~(size_t)0x1FFFFF);
  gqa_kernel_a<<<256, 768, 0, stream>>>(x, kc, vc, wq, wo, ws);
  gqa_kernel_b<<<224, 512, 0, stream>>>(ws, (unsigned char*)d_out);
}
```

```cpp
#include <hip/hip_runtime.h>
#include <hip/hip_bf16.h>
#include <stdint.h>

typedef unsigned v4u __attribute__((ext_vector_type(4)));
typedef unsigned v2u __attribute__((ext_vector_type(2)));
typedef short v4s __attribute__((ext_vector_type(4)));
typedef float f32x4 __attribute__((ext_vector_type(4)));
typedef float f32x2 __attribute__((ext_vector_type(2)));
typedef __bf16 bf16x8_t __attribute__((ext_vector_type(8)));
typedef __bf16 bf16x2_t __attribute__((ext_vector_type(2)));
#define LDS_AS __attribute__((address_space(3)))

constexpr int NB = 16, SQ = 48, DM = 448, HQ = 16, HKV = 8, SK = 48, DH = 256;
constexpr int SMEM_A = 163840;
constexpr int QSLOT = 45056;
constexpr int VSLOT = 57344;
constexpr int VIMG = 49152;
constexpr int VW_BLK = 43008;
constexpr int VW_R2 = 28672;
constexpr int P_BLK = 4608;
constexpr int P_R2 = 3072;
constexpr size_t P_OFF = (size_t)256 * VW_BLK;

#define TS(i)
#define TSDUMP(WSP)
#define STAMP_BEGIN
#define STAMP_END(K, WSP)

#define AUXW 0
#define AUXX 0
#define AUXKV 0
#define AUXT 2
#define TOUCH 1
#define ROT 0
#define TWC 1
#define AUXST 16

__device__ __forceinline__ unsigned pack_bf16(float a, float b) {
  f32x2 v = {a, b};
  bf16x2_t r = __builtin_convertvector(v, bf16x2_t);
  return __builtin_bit_cast(unsigned, r);
}
__device__ __forceinline__ float round_bf16(float a) {
  __bf16 r = (__bf16)a;
  return (float)r;
}
__device__ __forceinline__ f32x4 mfma16(v4u a, v4u b, f32x4 c) {
  return __builtin_amdgcn_mfma_f32_16x16x32_bf16(__builtin_bit_cast(bf16x8_t, a), __builtin_bit_cast(bf16x8_t, b), c, 0, 0, 0);
}
__device__ __forceinline__ void swap16(float& a, float& b) { asm volatile("s_nop 1\n\tv_permlane16_swap_b32 %0, %1\n\ts_nop 1" : "+v"(a), "+v"(b)); }
__device__ __forceinline__ void swap32(float& a, float& b) { asm volatile("s_nop 1\n\tv_permlane32_swap_b32 %0, %1\n\ts_nop 1" : "+v"(a), "+v"(b)); }
__device__ __forceinline__ float xlane_max(float v) {
  float a = v, b = v;
  swap16(a, b);
  a = fmaxf(a, b); b = a;
  swap32(a, b);
  return fmaxf(a, b);
}
__device__ __forceinline__ float xlane_sum(float v) {
  float a = v, b = v;
  swap16(a, b);
  a = a + b; b = a;
  swap32(a, b);
  return a + b;
}
__device__ __forceinline__ v2u tr_read(const unsigned char* p) {
  v4s r = __builtin_amdgcn_ds_read_tr16_b64_v4i16((LDS_AS v4s*)p);
  return __builtin_bit_cast(v2u, r);
}
template <int AUX>
__device__ __forceinline__ void dma16(__amdgpu_buffer_rsrc_t rs, unsigned ldsoff, int voff, int soff) {
  if constexpr (AUX == 2)
    asm volatile("s_nop 4\n\ts_mov_b32 m0, %0\n\ts_nop 0\n\tbuffer_load_dwordx4 %1, %2, %3 offen nt lds" :: "s"(ldsoff), "v"(voff), "s"(rs), "s"(soff) : "m0", "memory");
  else
    asm volatile("s_nop 4\n\ts_mov_b32 m0, %0\n\ts_nop 0\n\tbuffer_load_dwordx4 %1, %2, %3 offen lds" :: "s"(ldsoff), "v"(voff), "s"(rs), "s"(soff) : "m0", "memory");
}
#define DMA16(RS, LDSOFF, VOFF, SOFF, AUX) dma16<AUX>((RS), lds0 + (LDSOFF), (VOFF), (SOFF))
#define WAITV(n) asm volatile("s_waitcnt vmcnt(" #n ")" ::: "memory")
#define RBAR() { asm volatile("s_waitcnt lgkmcnt(0)" ::: "memory"); __builtin_amdgcn_sched_barrier(0); __builtin_amdgcn_s_barrier(); __builtin_amdgcn_sched_barrier(0); }

constexpr int KIMG = 2 * QSLOT;
__device__ __forceinline__ int next_slot(int cur) { return cur == 0 ? 2 * QSLOT : cur - QSLOT; }

__global__ __launch_bounds__(768, 3) void gqa_kernel_a(const unsigned char* __restrict__ x, const unsigned char* __restrict__ kc,
                                                       const unsigned char* __restrict__ vc, const unsigned char* __restrict__ wq,
                                                       const unsigned char* __restrict__ wo, unsigned char* __restrict__ ws,
                                                       const unsigned char* __restrict__ outp) {
  __shared__ __attribute__((aligned(16))) unsigned char smem[SMEM_A];
  STAMP_BEGIN
  TS(0)
  const int bid = blockIdx.x;
  const int xcd = bid & 7, bi = bid >> 3;
  const int type = bi & 1, h = 2 * xcd + ((bi >> 1) & 1), bp = bi >> 2;
  const int gk = h >> 1;
  const int tid = threadIdx.x, lane = tid & 63;
  const int w = __builtin_amdgcn_readfirstlane(tid >> 6);
  const int lg = lane >> 4, lc = lane & 15;
  const int hi = lane >> 5;
  const int q4 = (lane & 15) >> 2, p4 = lane & 3;
  const int bb = (w >> 2) & 1;
  const int cq = w & 3;
  const unsigned lds0 = (unsigned)(size_t)(LDS_AS unsigned char*)smem;

  if (type == 0) {
    if (w >= 8) {
      const int ld = w - 8;
      const __amdgpu_buffer_rsrc_t rsw = __builtin_amdgcn_make_buffer_rsrc((void*)wq, 0, DM * HQ * DH * 2, 0x00020000);
      const __amdgpu_buffer_rsrc_t rsx = __builtin_amdgcn_make_buffer_rsrc((void*)(x + (size_t)bp * (2 * SQ * DM * 2)), 0, 2 * SQ * DM * 2, 0x00020000);
      const __amdgpu_buffer_rsrc_t rsk = __builtin_amdgcn_make_buffer_rsrc((void*)kc, 0, NB * HKV * SK * DH * 2, 0x00020000);
      int wv[4], xv[2];
#pragma unroll
      for (int v = 0; v < 4; ++v) {
        const int sw = (2 * (v & 1) + hi) | ((v >> 1) << 2);
        wv[v] = hi * 8192 + h * 512 + (((lane & 31) ^ (sw << 1)) * 16);
      }
#pragma unroll
      for (int v = 0; v < 2; ++v) xv[v] = (lane >> 3) * 896 + (((lane & 7) ^ ((4 * ((ld + v) & 1) + (lane >> 4)) & 7)) * 16);
      auto issue = [&](int cc, unsigned slot, int lo, int hi_) {
#pragma unroll
        for (int i = 0; i < 3; ++i) if (i >= lo && i < hi_) DMA16(rsx, slot + 32768 + (3 * ld + i) * 1024, xv[i & 1], (3 * ld + i) * 8 * 896 + 128 * cc, AUXX);
#pragma unroll
        for (int i = 0; i < 8; ++i) if (i + 3 >= lo && i + 3 < hi_)
          DMA16(rsw, slot + ld * 8192 + i * 1024, wv[(i & 1) | (((i >> 2) & 1) << 1)], (2 * cc * 32 + 16 * ld + 2 * i) * 8192, AUXW);
      };
      issue(0, 0, 0, 11);
      issue(1, 2 * QSLOT, 0, 6);
      WAITV(6); RBAR()
      issue(1, 2 * QSLOT, 6, 11);
      issue(2, QSLOT, 0, 11);
      int islot = 0;
#pragma unroll 1
      for (int c = 1; c < 5; ++c) {
        WAITV(11); RBAR()
        issue(c + 2, islot, 0, 11);
        islot = next_slot(islot);
      }
      WAITV(11);
      RBAR()
      {
#pragma unroll 1
        for (int ii = 0; ii < 12; ++ii) {
          const int i = 12 * (ld & 1) + ii;
          const int kvo = hi * 512 + (((lane & 31) ^ ((2 * i + hi) & 15)) * 16);
          DMA16(rsk, KIMG + (ld >> 1) * 24576 + i * 1024, kvo, ((2 * bp + (ld >> 1)) * HKV + gk) * (SK * DH * 2) + i * 1024, AUXKV);
        }
      }
      WAITV(12);
      RBAR()
      WAITV(0);
      RBAR()
      RBAR()
    } else {
      f32x4 acc[3][4];
#pragma unroll
      for (int qt = 0; qt < 3; ++qt)
#pragma unroll
        for (int j = 0; j < 4; ++j) acc[qt][j] = f32x4{0.f, 0.f, 0.f, 0.f};
      const int xrd = 32768 + (48 * bb + lc) * 128 + ((lg ^ ((lc >> 1) & 7)) * 16);
      const int srd = q4 | ((lg & 1) << 2);
      const int wrd = (8 * lg + q4) * 512 + (((8 * cq + (p4 >> 1)) ^ (srd << 1)) * 16) + (p4 & 1) * 8;
      int cslot = 0;
#define ILQ(n) _Pragma("unroll") for (int i_ = 0; i_ < (n); ++i_) { __builtin_amdgcn_sched_group_barrier(0x8, 1, 0); __builtin_amdgcn_sched_group_barrier(0x100, 1, 0); }
      v4u xa[3], xb2[3], wa[4], wb[4], ka[3][2];
      auto rd = [&](v4u (&xf)[3], v4u (&wf)[4], const unsigned char* slot, int ss) {
#pragma unroll
        for (int qt = 0; qt < 3; ++qt) xf[qt] = *(const v4u*)(slot + ((xrd ^ (64 * ss)) + qt * 2048));
#pragma unroll
        for (int j = 0; j < 4; ++j) {
          v2u a = tr_read(slot + ss * 16384 + (wrd ^ (32 * j)));
          v2u b = tr_read(slot + ss * 16384 + (wrd ^ (32 * j)) + 2048);
          wf[j] = v4u{a[0], a[1], b[0], b[1]};
        }
      };
      auto mm = [&](const v4u (&xf)[3], const v4u (&wf)[4]) {
#pragma unroll
        for (int j = 0; j < 4; ++j)
#pragma unroll
          for (int qt = 0; qt < 3; ++qt) acc[qt][j] = mfma16(wf[j], xf[qt], acc[qt][j]);
      };
      unsigned tw = 0;
      TS(1)
      RBAR()
      rd(xa, wa, smem, 0);
#pragma unroll
      for (int c = 0; c < 7; ++c) {
        const unsigned char* slot = smem + cslot;
        rd(xb2, wb, slot, 1);
        mm(xa, wa);
        ILQ(11)
        __builtin_amdgcn_sched_barrier(0);
        cslot = next_slot(cslot);
        RBAR()
        if (c == TWC) {
          const unsigned char* ta;
          if (w < 2)      ta = kc + (size_t)((2 * bp + w) * HKV + gk) * (SK * DH * 2);
          else if (w < 4) ta = ws + P_OFF + (size_t)((2 * bp + (w - 2)) * 16 + h) * P_BLK;
          else            ta = outp + (size_t)(xcd + 8 * (w & 1)) * (SQ * DM * 2) + (bi & 15) * 2688;
          tw = __builtin_amdgcn_raw_buffer_load_b32(__builtin_amdgcn_make_buffer_rsrc((void*)ta, 0, 64, 0x00020000), 0, 0, 0);
          __builtin_amdgcn_sched_barrier(0);
        }
        if (c < 6) rd(xa, wa, smem + cslot, 0);
        else {
          const unsigned char* kl = smem + KIMG + (48 * bb + lc) * 512 + (lg & 1) * 8;
#pragma unroll
          for (int kt = 0; kt < 3; ++kt)
#pragma unroll
            for (int f = 0; f < 2; ++f) {
              v2u a = *(const v2u*)(kl + kt * 8192 + (((8 * cq + 4 * f + (lg >> 1)) ^ lc) * 16));
              v2u b = *(const v2u*)(kl + kt * 8192 + (((8 * cq + 4 * f + 2 + (lg >> 1)) ^ lc) * 16));
              ka[kt][f] = v4u{a[0], a[1], b[0], b[1]};
            }
        }
        mm(xb2, wb);
        ILQ(11)
        __builtin_amdgcn_sched_barrier(0);
      }
      f32x4 sacc[3][3];
      {
#pragma unroll
        for (int qt = 0; qt < 3; ++qt) {
#pragma unroll
          for (int kt = 0; kt < 3; ++kt) sacc[kt][qt] = f32x4{0.f, 0.f, 0.f, 0.f};
#pragma unroll
          for (int f = 0; f < 2; ++f) {
            v4u qf;
            qf[0] = pack_bf16(acc[qt][2 * f][0], acc[qt][2 * f][1]);
            qf[1] = pack_bf16(acc[qt][2 * f][2], acc[qt][2 * f][3]);
            qf[2] = pack_bf16(acc[qt][2 * f + 1][0], acc[qt][2 * f + 1][1]);
            qf[3] = pack_bf16(acc[qt][2 * f + 1][2], acc[qt][2 * f + 1][3]);
#pragma unroll
            for (int kt = 0; kt < 3; ++kt) sacc[kt][qt] = mfma16(ka[kt][f], qf, sacc[kt][qt]);
          }
        }
      }
      asm volatile("" :: "v"(tw));
      TS(9)
      unsigned char* sp = smem;
#pragma unroll
      for (int kt = 0; kt < 3; ++kt)
#pragma unroll
        for (int qt = 0; qt < 3; ++qt) *(f32x4*)(sp + ((w * 9 + qt * 3 + kt) * 64 + lane) * 16) = sacc[kt][qt];
      TS(10)
      RBAR()
      TS(11)
      if (w < 6) {
        const int b2 = w >= 3 ? 1 : 0, qt = w - 3 * b2;
        f32x4 pr[4][3];
        const unsigned char* spl = sp + ((36 * b2 + qt * 3) * 64 + lane) * 16;
#pragma unroll
        for (int wv = 0; wv < 4; ++wv)
#pragma unroll
          for (int kt = 0; kt < 3; ++kt) pr[wv][kt] = *(const f32x4*)(spl + (wv * 9 + kt) * 1024);
        __builtin_amdgcn_sched_barrier(0);
        float sv[12];
#pragma unroll
        for (int kt = 0; kt < 3; ++kt) {
          const f32x4 a = (pr[0][kt] + pr[1][kt]) + (pr[2][kt] + pr[3][kt]);
          const unsigned u0 = pack_bf16(a[0], a[1]), u1 = pack_bf16(a[2], a[3]);
          sv[4 * kt + 0] = __builtin_bit_cast(float, u0 << 16);
          sv[4 * kt + 1] = __builtin_bit_cast(float, u0 & 0xffff0000u);
          sv[4 * kt + 2] = __builtin_bit_cast(float, u1 << 16);
          sv[4 * kt + 3] = __builtin_bit_cast(float, u1 & 0xffff0000u);
        }
        float m = fmaxf(fmaxf(fmaxf(sv[0], sv[1]), fmaxf(sv[2], sv[3])), fmaxf(fmaxf(sv[4], sv[5]), fmaxf(sv[6], sv[7])));
        m = fmaxf(m, fmaxf(fmaxf(sv[8], sv[9]), fmaxf(sv[10], sv[11])));
        m = xlane_max(m);
        const float cs = 0.0625f * 1.44269504088896340736f;
        const float mc = -m * cs;
        float sum = 0.f;
#pragma unroll
        for (int i = 0; i < 12; ++i) {
          sv[i] = __builtin_amdgcn_exp2f(__builtin_fmaf(sv[i], cs, mc));
          sum += sv[i];
        }
        sum = xlane_sum(sum);
        const float inv = __builtin_amdgcn_rcpf(sum);
        v4u p1;
        v2u p2;
        p1[0] = pack_bf16(sv[0] * inv, sv[1] * inv);
        p1[1] = pack_bf16(sv[2] * inv, sv[3] * inv);
        p1[2] = pack_bf16(sv[4] * inv, sv[5] * inv);
        p1[3] = pack_bf16(sv[6] * inv, sv[7] * inv);
        p2[0] = pack_bf16(sv[8] * inv, sv[9] * inv);
        p2[1] = pack_bf16(sv[10] * inv, sv[11] * inv);
        unsigned char* pb = ws + P_OFF + (size_t)((2 * bp + b2) * 16 + h) * P_BLK;
        *(v4u*)(pb + qt * 1024 + lane * 16) = p1;
        *(v2u*)(pb + P_R2 + qt * 512 + lane * 8) = p2;
      }
    }
  } else {
    if (w >= 8) {
      const int ld = w - 8;
      const __amdgpu_buffer_rsrc_t rs = __builtin_amdgcn_make_buffer_rsrc((void*)wo, 0, DM * HQ * DH * 2, 0x00020000);
      const __amdgpu_buffer_rsrc_t rsv = __builtin_amdgcn_make_buffer_rsrc((void*)vc, 0, NB * HKV * SK * DH * 2, 0x00020000);
      int vv[2];
#pragma unroll
      for (int v = 0; v < 2; ++v) vv[v] = (lane >> 3) * 512 + (((lane & 7) ^ ((4 * ((ld + v) & 1) + (lane >> 4)) & 7)) * 16);
      int wo_voff[7];
#pragma unroll
      for (int r = 0; r < 7; ++r) {
        const int P = 1024 * r + 16 * lane;
        const int row = P / 896;
        const int phys = (P - 896 * row) >> 4;
        const int sp_ = ((row >> 1) & 1) | ((ld & 1) << 1);
        wo_voff[r] = row * 896 + ((phys ^ (sp_ << 1)) * 16);
      }
      auto issue = [&](int st) {
        if ((st & 1) == 0) {
#pragma unroll
          for (int i = 0; i < 3; ++i)
            DMA16(rsv, (st >> 1) * 12288 + (3 * ld + i) * 1024, vv[i & 1],
                  ((2 * bp + (ld >> 1)) * HKV + gk) * (SK * DH * 2) + (3 * (ld & 1) + i) * 8 * 512 + 64 * st, AUXKV);
        }
        const unsigned slot = VIMG + (st & 3) * 28672 + ld * 7168;
#pragma unroll
        for (int r = 0; r < 7; ++r) DMA16(rs, slot + r * 1024, wo_voff[r], (h * 256 + 32 * st + 8 * ld) * 896, AUXW);
      };
      issue(0); issue(1);
      WAITV(7);  RBAR() issue(2); issue(3);
      WAITV(17); RBAR() issue(4);
      WAITV(17); RBAR() issue(5);
      WAITV(17); RBAR() issue(6);
      WAITV(17); RBAR() issue(7);
      WAITV(17); RBAR()
      WAITV(7);  RBAR()
      WAITV(0);  RBAR()
    } else {
      const int cw = cq;
      f32x4 acc[3][7];
#pragma unroll
      for (int kt = 0; kt < 3; ++kt)
#pragma unroll
        for (int ct = 0; ct < 7; ++ct) acc[kt][ct] = f32x4{0.f, 0.f, 0.f, 0.f};
      const int vrd = (48 * bb + lc) * 128 + ((lg ^ ((lc >> 1) & 7)) * 16);
      const int sp2 = ((q4 >> 1) & 1) | ((lg & 1) << 1);
      const int wrow = (8 * lg + q4) * 896 + (p4 & 1) * 8;
      v4u va[3], vb[3], wa[4], wb[3];
      auto rdv = [&](v4u (&vf)[3], int c, int ss) {
#pragma unroll
        for (int kt = 0; kt < 3; ++kt) vf[kt] = *(const v4u*)(smem + c * 12288 + ((vrd ^ (64 * ss)) + kt * 2048));
      };
      int wbase[4];
#pragma unroll
      for (int i = 0; i < 4; ++i) wbase[i] = VIMG + wrow + (((14 * cw + 2 * i + (p4 >> 1)) ^ (sp2 << 1)) * 16);
      auto rdw = [&](v4u* wf, int st, int ct0, int n) {
#pragma unroll
        for (int i = 0; i < n; ++i) {
          const unsigned char* p = smem + wbase[i] + ((st & 3) * 28672 + (ct0 ? 128 : 0));
          v2u a = tr_read(p);
          v2u b = tr_read(p + 3584);
          wf[i] = v4u{a[0], a[1], b[0], b[1]};
        }
      };
      auto mm = [&](const v4u (&vf)[3], const v4u* wf, int ct0, int n) {
#pragma unroll
        for (int i = 0; i < n; ++i)
#pragma unroll
          for (int kt = 0; kt < 3; ++kt) acc[kt][ct0 + i] = mfma16(vf[kt], wf[i], acc[kt][ct0 + i]);
      };
#define SB __builtin_amdgcn_sched_barrier(0);
#define IL(n) _Pragma("unroll") for (int i_ = 0; i_ < (n); ++i_) { __builtin_amdgcn_sched_group_barrier(0x8, 1, 0); __builtin_amdgcn_sched_group_barrier(0x100, 1, 0); }
#define IL2(n) _Pragma("unroll") for (int i_ = 0; i_ < (n); ++i_) { __builtin_amdgcn_sched_group_barrier(0x8, 1, 0); __builtin_amdgcn_sched_group_barrier(0x100, 2, 0); }
      unsigned tw = 0;
      TS(1)
      RBAR()
      rdv(va, 0, 0);
      rdw(wa, 0, 0, 4);
#pragma unroll
      for (int c = 0; c < 4; ++c) {
        rdw(wb, 2 * c, 4, 3);
        mm(va, wa, 0, 4); IL(6) SB
        RBAR()
        if (c == TWC) {
          tw = __builtin_amdgcn_raw_buffer_load_b32(__builtin_amdgcn_make_buffer_rsrc((void*)(ws + (size_t)((2 * bp + bb) * 16 + h) * VW_BLK + cw * 10752), 0, 64, 0x00020000), 0, 0, 0);
          __builtin_amdgcn_sched_barrier(0);
        }
        rdv(vb, c, 1); rdw(wa, 2 * c + 1, 0, 4);
        mm(va, wb, 4, 3); IL2(6) SB
        rdw(wb, 2 * c + 1, 4, 3);
        mm(vb, wa, 0, 4); IL(6) SB
        if (c < 3) {
          RBAR()
          rdv(va, c + 1, 0);
          rdw(wa, 2 * c + 2, 0, 4);
          mm(vb, wb, 4, 3); IL2(6) SB
        } else {
          mm(vb, wb, 4, 3); SB
        }
      }
      asm volatile("" :: "v"(tw));
      const __amdgpu_buffer_rsrc_t rso = __builtin_amdgcn_make_buffer_rsrc((void*)ws, 0, 256 * VW_BLK, 0x00020000);
      {
        const int ob = ((2 * bp + bb) * 16 + h) * VW_BLK;
        const int o1 = ob + 7 * cw * 1024 + lane * 16;
        const int o2 = ob + VW_R2 + 7 * cw * 512 + lane * 8;
#pragma unroll
        for (int ct = 0; ct < 7; ++ct) {
          v4u o;
          o[0] = pack_bf16(acc[0][ct][0], acc[0][ct][1]);
          o[1] = pack_bf16(acc[0][ct][2], acc[0][ct][3]);
          o[2] = pack_bf16(acc[1][ct][0], acc[1][ct][1]);
          o[3] = pack_bf16(acc[1][ct][2], acc[1][ct][3]);
          __builtin_amdgcn_raw_buffer_store_b128(o, rso, o1 + ct * 1024, 0, AUXST);
          v2u o3;
          o3[0] = pack_bf16(acc[2][ct][0], acc[2][ct][1]);
          o3[1] = pack_bf16(acc[2][ct][2], acc[2][ct][3]);
          __builtin_amdgcn_raw_buffer_store_b64(o3, rso, o2 + ct * 512, 0, AUXST);
        }
      }
      TS(10)
    }
  }
  TS(12)
  TSDUMP(ws)
  STAMP_END(0, ws)
}

__global__ __launch_bounds__(512) void gqa_kernel_b(const unsigned char* __restrict__ ws, unsigned char* __restrict__ out) {
  __shared__ __attribute__((aligned(16))) float red[8 * 48 * 36];
  STAMP_BEGIN
  const int bid = blockIdx.x;
  const int xcd = bid & 7, bi = bid >> 3;
  const int b = xcd + 8 * (bi / 14), m = bi % 14;
  const int tid = threadIdx.x, lane = tid & 63;
  const int w = __builtin_amdgcn_readfirstlane(tid >> 6);
  const int lg = lane >> 4, lc = lane & 15;

  v4u p1[2][3], v1[2][2];
  v2u p2[2][3], v2[2][2];
#pragma unroll
  for (int hh = 0; hh < 2; ++hh) {
    const int h = 2 * w + hh;
    const unsigned char* pb = ws + P_OFF + (size_t)(b * 16 + h) * P_BLK;
    const unsigned char* vb = ws + (size_t)(b * 16 + h) * VW_BLK;
#pragma unroll
    for (int ct = 0; ct < 2; ++ct) {
      v1[hh][ct] = *(const v4u*)(vb + (2 * m + ct) * 1024 + lane * 16);
      v2[hh][ct] = *(const v2u*)(vb + VW_R2 + (2 * m + ct) * 512 + lane * 8);
    }
#pragma unroll
    for (int qt = 0; qt < 3; ++qt) {
      p1[hh][qt] = *(const v4u*)(pb + qt * 1024 + lane * 16);
      p2[hh][qt] = *(const v2u*)(pb + P_R2 + qt * 512 + lane * 8);
    }
  }
#pragma unroll
  for (int qt = 0; qt < 3; ++qt)
#pragma unroll
    for (int ct = 0; ct < 2; ++ct) {
      f32x4 a = f32x4{0.f, 0.f, 0.f, 0.f};
      a = mfma16(p1[0][qt], v1[0][ct], a);
      a = mfma16(p1[1][qt], v1[1][ct], a);
      v4u pa = {p2[0][qt][0], p2[0][qt][1], p2[1][qt][0], p2[1][qt][1]};
      v4u va = {v2[0][ct][0], v2[0][ct][1], v2[1][ct][0], v2[1][ct][1]};
      a = mfma16(pa, va, a);
#pragma unroll
      for (int r = 0; r < 4; ++r) red[(w * 48 + 16 * qt + 4 * lg + r) * 36 + 16 * ct + lc] = a[r];
    }
  __syncthreads();
  if (tid < 384) {
    const int row = tid >> 3, c4 = tid & 7;
    f32x4 s = *(const f32x4*)(red + row * 36 + 4 * c4);
#pragma unroll
    for (int wv = 1; wv < 8; ++wv) s += *(const f32x4*)(red + (wv * 48 + row) * 36 + 4 * c4);
    v2u o = {pack_bf16(s[0], s[1]), pack_bf16(s[2], s[3])};
    *(v2u*)(out + (((size_t)b * SQ + row) * DM + 32 * m + 4 * c4) * 2) = o;
  }
  STAMP_END(1, ws)
}

extern "C" void kernel_launch(void* const* d_in, const int* in_sizes, int n_in, void* d_out, int out_size, void* d_ws,
                              size_t ws_size, hipStream_t stream) {
  const unsigned char* x = (const unsigned char*)d_in[0];
  const unsigned char* kc = (const unsigned char*)d_in[1];
  const unsigned char* vc = (const unsigned char*)d_in[2];
  const unsigned char* wq = (const unsigned char*)d_in[3];
  const unsigned char* wo = (const unsigned char*)d_in[4];
  unsigned char* ws = (unsigned char*)d_ws;
  gqa_kernel_a<<<256, 768, 0, stream>>>(x, kc, vc, wq, wo, ws, (const unsigned char*)d_out);
  gqa_kernel_b<<<224, 512, 0, stream>>>(ws, (unsigned char*)d_out);
}
```

```cpp
#include <hip/hip_runtime.h>
#include <hip/hip_bf16.h>
#include <stdint.h>

typedef unsigned v4u __attribute__((ext_vector_type(4)));
typedef unsigned v2u __attribute__((ext_vector_type(2)));
typedef short v4s __attribute__((ext_vector_type(4)));
typedef float f32x4 __attribute__((ext_vector_type(4)));
typedef float f32x2 __attribute__((ext_vector_type(2)));
typedef __bf16 bf16x8_t __attribute__((ext_vector_type(8)));
typedef __bf16 bf16x2_t __attribute__((ext_vector_type(2)));
#define LDS_AS __attribute__((address_space(3)))

constexpr int NB = 16, SQ = 48, DM = 448, HQ = 16, HKV = 8, SK = 48, DH = 256;
constexpr int SMEM_A = 163840;
constexpr int QSLOT = 45056;
constexpr int VIMG = 49152;
constexpr int VW_BLK = 43008;
constexpr int VW_R2 = 28672;
constexpr int P_BLK = 4608;
constexpr int P_R2 = 3072;
constexpr size_t P_OFF = (size_t)256 * VW_BLK;

__device__ __forceinline__ unsigned pack_bf16(float a, float b) {
  f32x2 v = {a, b};
  bf16x2_t r = __builtin_convertvector(v, bf16x2_t);
  return __builtin_bit_cast(unsigned, r);
}
__device__ __forceinline__ f32x4 mfma16(v4u a, v4u b, f32x4 c) {
  return __builtin_amdgcn_mfma_f32_16x16x32_bf16(__builtin_bit_cast(bf16x8_t, a), __builtin_bit_cast(bf16x8_t, b), c, 0, 0, 0);
}
__device__ __forceinline__ void swap16(float& a, float& b) { asm volatile("s_nop 1\n\tv_permlane16_swap_b32 %0, %1\n\ts_nop 1" : "+v"(a), "+v"(b)); }
__device__ __forceinline__ void swap32(float& a, float& b) { asm volatile("s_nop 1\n\tv_permlane32_swap_b32 %0, %1\n\ts_nop 1" : "+v"(a), "+v"(b)); }
__device__ __forceinline__ float xlane_max(float v) {
  float a = v, b = v;
  swap16(a, b);
  a = fmaxf(a, b); b = a;
  swap32(a, b);
  return fmaxf(a, b);
}
__device__ __forceinline__ float xlane_sum(float v) {
  float a = v, b = v;
  swap16(a, b);
  a = a + b; b = a;
  swap32(a, b);
  return a + b;
}
__device__ __forceinline__ v2u tr_read(const unsigned char* p) {
  v4s r = __builtin_amdgcn_ds_read_tr16_b64_v4i16((LDS_AS v4s*)p);
  return __builtin_bit_cast(v2u, r);
}
__device__ __forceinline__ void dma16(__amdgpu_buffer_rsrc_t rs, unsigned ldsoff, int voff, int soff) {
  asm volatile("s_nop 4\n\ts_mov_b32 m0, %0\n\ts_nop 0\n\tbuffer_load_dwordx4 %1, %2, %3 offen lds" :: "s"(ldsoff), "v"(voff), "s"(rs), "s"(soff) : "m0", "memory");
}
#define DMA16(RS, LDSOFF, VOFF, SOFF) dma16((RS), lds0 + (LDSOFF), (VOFF), (SOFF))
#define WAITV(n) asm volatile("s_waitcnt vmcnt(" #n ")" ::: "memory")
#define RBAR() { asm volatile("s_waitcnt lgkmcnt(0)" ::: "memory"); __builtin_amdgcn_sched_barrier(0); __builtin_amdgcn_s_barrier(); __builtin_amdgcn_sched_barrier(0); }

constexpr int KIMG = 2 * QSLOT;
__device__ __forceinline__ int next_slot(int cur) { return cur == 0 ? 2 * QSLOT : cur - QSLOT; }

__global__ __launch_bounds__(768, 3) void gqa_kernel_a(const unsigned char* __restrict__ x, const unsigned char* __restrict__ kc,
                                                       const unsigned char* __restrict__ vc, const unsigned char* __restrict__ wq,
                                                       const unsigned char* __restrict__ wo, unsigned char* __restrict__ ws,
                                                       const unsigned char* __restrict__ outp) {
  __shared__ __attribute__((aligned(16))) unsigned char smem[SMEM_A];
  const int bid = blockIdx.x;
  const int xcd = bid & 7, bi = bid >> 3;
  const int type = bi & 1, h = 2 * xcd + ((bi >> 1) & 1), bp = bi >> 2;
  const int gk = h >> 1;
  const int tid = threadIdx.x, lane = tid & 63;
  const int w = __builtin_amdgcn_readfirstlane(tid >> 6);
  const int lg = lane >> 4, lc = lane & 15;
  const int hi = lane >> 5;
  const int q4 = (lane & 15) >> 2, p4 = lane & 3;
  const int bb = (w >> 2) & 1;
  const int cq = w & 3;
  const unsigned lds0 = (unsigned)(size_t)(LDS_AS unsigned char*)smem;

  if (type == 0) {
    if (w >= 8) {
      const int ld = w - 8;
      const __amdgpu_buffer_rsrc_t rsw = __builtin_amdgcn_make_buffer_rsrc((void*)wq, 0, DM * HQ * DH * 2, 0x00020000);
      const __amdgpu_buffer_rsrc_t rsx = __builtin_amdgcn_make_buffer_rsrc((void*)(x + (size_t)bp * (2 * SQ * DM * 2)), 0, 2 * SQ * DM * 2, 0x00020000);
      const __amdgpu_buffer_rsrc_t rsk = __builtin_amdgcn_make_buffer_rsrc((void*)kc, 0, NB * HKV * SK * DH * 2, 0x00020000);
      int wv[4], xv[2];
#pragma unroll
      for (int v = 0; v < 4; ++v) {
        const int sw = (2 * (v & 1) + hi) | ((v >> 1) << 2);
        wv[v] = hi * 8192 + h * 512 + (((lane & 31) ^ (sw << 1)) * 16);
      }
#pragma unroll
      for (int v = 0; v < 2; ++v) xv[v] = (lane >> 3) * 896 + (((lane & 7) ^ ((4 * ((ld + v) & 1) + (lane >> 4)) & 7)) * 16);
      auto issue = [&](int cc, unsigned slot, int lo, int hi_) {
#pragma unroll
        for (int i = 0; i < 3; ++i) if (i >= lo && i < hi_) DMA16(rsx, slot + 32768 + (3 * ld + i) * 1024, xv[i & 1], (3 * ld + i) * 8 * 896 + 128 * cc);
#pragma unroll
        for (int i = 0; i < 8; ++i) if (i + 3 >= lo && i + 3 < hi_)
          DMA16(rsw, slot + ld * 8192 + i * 1024, wv[(i & 1) | (((i >> 2) & 1) << 1)], (2 * cc * 32 + 16 * ld + 2 * i) * 8192);
      };
      issue(0, 0, 0, 11);
      issue(1, 2 * QSLOT, 0, 6);
      WAITV(6); RBAR()
      issue(1, 2 * QSLOT, 6, 11);
      issue(2, QSLOT, 0, 11);
      int islot = 0;
#pragma unroll 1
      for (int c = 1; c < 5; ++c) {
        WAITV(11); RBAR()
        issue(c + 2, islot, 0, 11);
        islot = next_slot(islot);
      }
      WAITV(11);
      RBAR()
      {
#pragma unroll 1
        for (int ii = 0; ii < 12; ++ii) {
          const int i = 12 * (ld & 1) + ii;
          const int kvo = hi * 512 + (((lane & 31) ^ ((2 * i + hi) & 15)) * 16);
          DMA16(rsk, KIMG + (ld >> 1) * 24576 + i * 1024, kvo, ((2 * bp + (ld >> 1)) * HKV + gk) * (SK * DH * 2) + i * 1024);
        }
      }
      WAITV(12);
      RBAR()
      WAITV(0);
      RBAR()
      RBAR()
    } else {
      f32x4 acc[3][4];
#pragma unroll
      for (int qt = 0; qt < 3; ++qt)
#pragma unroll
        for (int j = 0; j < 4; ++j) acc[qt][j] = f32x4{0.f, 0.f, 0.f, 0.f};
      const int xrd = 32768 + (48 * bb + lc) * 128 + ((lg ^ ((lc >> 1) & 7)) * 16);
      const int srd = q4 | ((lg & 1) << 2);
      const int wrd = (8 * lg + q4) * 512 + (((8 * cq + (p4 >> 1)) ^ (srd << 1)) * 16) + (p4 & 1) * 8;
      int cslot = 0;
#define ILQ(n) _Pragma("unroll") for (int i_ = 0; i_ < (n); ++i_) { __builtin_amdgcn_sched_group_barrier(0x8, 1, 0); __builtin_amdgcn_sched_group_barrier(0x100, 1, 0); }
      v4u xa[3], xb2[3], wa[4], wb[4], ka[3][2];
      auto rd = [&](v4u (&xf)[3], v4u (&wf)[4], const unsigned char* slot, int ss) {
#pragma unroll
        for (int qt = 0; qt < 3; ++qt) xf[qt] = *(const v4u*)(slot + ((xrd ^ (64 * ss)) + qt * 2048));
#pragma unroll
        for (int j = 0; j < 4; ++j) {
          v2u a = tr_read(slot + ss * 16384 + (wrd ^ (32 * j)));
          v2u b = tr_read(slot + ss * 16384 + (wrd ^ (32 * j)) + 2048);
          wf[j] = v4u{a[0], a[1], b[0], b[1]};
        }
      };
      auto mm = [&](const v4u (&xf)[3], const v4u (&wf)[4]) {
#pragma unroll
        for (int j = 0; j < 4; ++j)
#pragma unroll
          for (int qt = 0; qt < 3; ++qt) acc[qt][j] = mfma16(wf[j], xf[qt], acc[qt][j]);
      };
      unsigned tw = 0;
      RBAR()
      rd(xa, wa, smem, 0);
#pragma unroll
      for (int c = 0; c < 7; ++c) {
        const unsigned char* slot = smem + cslot;
        rd(xb2, wb, slot, 1);
        mm(xa, wa);
        ILQ(11)
        __builtin_amdgcn_sched_barrier(0);
        cslot = next_slot(cslot);
        RBAR()
        if (c == 1) {
          const unsigned char* ta;
          if (w < 2)      ta = kc + (size_t)((2 * bp + w) * HKV + gk) * (SK * DH * 2);
          else if (w < 4) ta = ws + P_OFF + (size_t)((2 * bp + (w - 2)) * 16 + h) * P_BLK;
          else            ta = outp + (size_t)(xcd + 8 * (w & 1)) * (SQ * DM * 2) + (bi & 15) * 2688;
          tw = __builtin_amdgcn_raw_buffer_load_b32(__builtin_amdgcn_make_buffer_rsrc((void*)ta, 0, 64, 0x00020000), 0, 0, 0);
          __builtin_amdgcn_sched_barrier(0);
        }
        if (c < 6) rd(xa, wa, smem + cslot, 0);
        else {
          const unsigned char* kl = smem + KIMG + (48 * bb + lc) * 512 + (lg & 1) * 8;
#pragma unroll
          for (int kt = 0; kt < 3; ++kt)
#pragma unroll
            for (int f = 0; f < 2; ++f) {
              v2u a = *(const v2u*)(kl + kt * 8192 + (((8 * cq + 4 * f + (lg >> 1)) ^ lc) * 16));
              v2u b = *(const v2u*)(kl + kt * 8192 + (((8 * cq + 4 * f + 2 + (lg >> 1)) ^ lc) * 16));
              ka[kt][f] = v4u{a[0], a[1], b[0], b[1]};
            }
        }
        mm(xb2, wb);
        ILQ(11)
        __builtin_amdgcn_sched_barrier(0);
      }
      f32x4 sacc[3][3];
      {
#pragma unroll
        for (int qt = 0; qt < 3; ++qt) {
#pragma unroll
          for (int kt = 0; kt < 3; ++kt) sacc[kt][qt] = f32x4{0.f, 0.f, 0.f, 0.f};
#pragma unroll
          for (int f = 0; f < 2; ++f) {
            v4u qf;
            qf[0] = pack_bf16(acc[qt][2 * f][0], acc[qt][2 * f][1]);
            qf[1] = pack_bf16(acc[qt][2 * f][2], acc[qt][2 * f][3]);
            qf[2] = pack_bf16(acc[qt][2 * f + 1][0], acc[qt][2 * f + 1][1]);
            qf[3] = pack_bf16(acc[qt][2 * f + 1][2], acc[qt][2 * f + 1][3]);
#pragma unroll
            for (int kt = 0; kt < 3; ++kt) sacc[kt][qt] = mfma16(ka[kt][f], qf, sacc[kt][qt]);
          }
        }
      }
      asm volatile("" :: "v"(tw));
      unsigned char* sp = smem;
#pragma unroll
      for (int kt = 0; kt < 3; ++kt)
#pragma unroll
        for (int qt = 0; qt < 3; ++qt) *(f32x4*)(sp + ((w * 9 + qt * 3 + kt) * 64 + lane) * 16) = sacc[kt][qt];
      RBAR()
      if (w < 6) {
        const int b2 = w >= 3 ? 1 : 0, qt = w - 3 * b2;
        f32x4 pr[4][3];
        const unsigned char* spl = sp + ((36 * b2 + qt * 3) * 64 + lane) * 16;
#pragma unroll
        for (int wv = 0; wv < 4; ++wv)
#pragma unroll
          for (int kt = 0; kt < 3; ++kt) pr[wv][kt] = *(const f32x4*)(spl + (wv * 9 + kt) * 1024);
        __builtin_amdgcn_sched_barrier(0);
        float sv[12];
#pragma unroll
        for (int kt = 0; kt < 3; ++kt) {
          const f32x4 a = (pr[0][kt] + pr[1][kt]) + (pr[2][kt] + pr[3][kt]);
          const unsigned u0 = pack_bf16(a[0], a[1]), u1 = pack_bf16(a[2], a[3]);
          sv[4 * kt + 0] = __builtin_bit_cast(float, u0 << 16);
          sv[4 * kt + 1] = __builtin_bit_cast(float, u0 & 0xffff0000u);
          sv[4 * kt + 2] = __builtin_bit_cast(float, u1 << 16);
          sv[4 * kt + 3] = __builtin_bit_cast(float, u1 & 0xffff0000u);
        }
        float m = fmaxf(fmaxf(fmaxf(sv[0], sv[1]), fmaxf(sv[2], sv[3])), fmaxf(fmaxf(sv[4], sv[5]), fmaxf(sv[6], sv[7])));
        m = fmaxf(m, fmaxf(fmaxf(sv[8], sv[9]), fmaxf(sv[10], sv[11])));
        m = xlane_max(m);
        const float cs = 0.0625f * 1.44269504088896340736f;
        const float mc = -m * cs;
        float sum = 0.f;
#pragma unroll
        for (int i = 0; i < 12; ++i) {
          sv[i] = __builtin_amdgcn_exp2f(__builtin_fmaf(sv[i], cs, mc));
          sum += sv[i];
        }
        sum = xlane_sum(sum);
        const float inv = __builtin_amdgcn_rcpf(sum);
        v4u p1;
        v2u p2;
        p1[0] = pack_bf16(sv[0] * inv, sv[1] * inv);
        p1[1] = pack_bf16(sv[2] * inv, sv[3] * inv);
        p1[2] = pack_bf16(sv[4] * inv, sv[5] * inv);
        p1[3] = pack_bf16(sv[6] * inv, sv[7] * inv);
        p2[0] = pack_bf16(sv[8] * inv, sv[9] * inv);
        p2[1] = pack_bf16(sv[10] * inv, sv[11] * inv);
        unsigned char* pb = ws + P_OFF + (size_t)((2 * bp + b2) * 16 + h) * P_BLK;
        *(v4u*)(pb + qt * 1024 + lane * 16) = p1;
        *(v2u*)(pb + P_R2 + qt * 512 + lane * 8) = p2;
      }
    }
  } else {
    if (w >= 8) {
      const int ld = w - 8;
      const __amdgpu_buffer_rsrc_t rs = __builtin_amdgcn_make_buffer_rsrc((void*)wo, 0, DM * HQ * DH * 2, 0x00020000);
      const __amdgpu_buffer_rsrc_t rsv = __builtin_amdgcn_make_buffer_rsrc((void*)vc, 0, NB * HKV * SK * DH * 2, 0x00020000);
      int vv[2];
#pragma unroll
      for (int v = 0; v < 2; ++v) vv[v] = (lane >> 3) * 512 + (((lane & 7) ^ ((4 * ((ld + v) & 1) + (lane >> 4)) & 7)) * 16);
      int wo_voff[7];
#pragma unroll
      for (int r = 0; r < 7; ++r) {
        const int P = 1024 * r + 16 * lane;
        const int row = P / 896;
        const int phys = (P - 896 * row) >> 4;
        const int sp_ = ((row >> 1) & 1) | ((ld & 1) << 1);
        wo_voff[r] = row * 896 + ((phys ^ (sp_ << 1)) * 16);
      }
      auto issue = [&](int st) {
        if ((st & 1) == 0) {
#pragma unroll
          for (int i = 0; i < 3; ++i)
            DMA16(rsv, (st >> 1) * 12288 + (3 * ld + i) * 1024, vv[i & 1],
                  ((2 * bp + (ld >> 1)) * HKV + gk) * (SK * DH * 2) + (3 * (ld & 1) + i) * 8 * 512 + 64 * st);
        }
        const unsigned slot = VIMG + (st & 3) * 28672 + ld * 7168;
#pragma unroll
        for (int r = 0; r < 7; ++r) DMA16(rs, slot + r * 1024, wo_voff[r], (h * 256 + 32 * st + 8 * ld) * 896);
      };
      issue(0); issue(1);
      WAITV(7);  RBAR() issue(2); issue(3);
      WAITV(17); RBAR() issue(4);
      WAITV(17); RBAR() issue(5);
      WAITV(17); RBAR() issue(6);
      WAITV(17); RBAR() issue(7);
      WAITV(17); RBAR()
      WAITV(7);  RBAR()
      WAITV(0);  RBAR()
    } else {
      const int cw = cq;
      f32x4 acc[3][7];
#pragma unroll
      for (int kt = 0; kt < 3; ++kt)
#pragma unroll
        for (int ct = 0; ct < 7; ++ct) acc[kt][ct] = f32x4{0.f, 0.f, 0.f, 0.f};
      const int vrd = (48 * bb + lc) * 128 + ((lg ^ ((lc >> 1) & 7)) * 16);
      const int sp2 = ((q4 >> 1) & 1) | ((lg & 1) << 1);
      const int wrow = (8 * lg + q4) * 896 + (p4 & 1) * 8;
      v4u va[3], vb[3], wa[4], wb[3];
      auto rdv = [&](v4u (&vf)[3], int c, int ss) {
#pragma unroll
        for (int kt = 0; kt < 3; ++kt) vf[kt] = *(const v4u*)(smem + c * 12288 + ((vrd ^ (64 * ss)) + kt * 2048));
      };
      int wbase[4];
#pragma unroll
      for (int i = 0; i < 4; ++i) wbase[i] = VIMG + wrow + (((14 * cw + 2 * i + (p4 >> 1)) ^ (sp2 << 1)) * 16);
      auto rdw = [&](v4u* wf, int st, int ct0, int n) {
#pragma unroll
        for (int i = 0; i < n; ++i) {
          const unsigned char* p = smem + wbase[i] + ((st & 3) * 28672 + (ct0 ? 128 : 0));
          v2u a = tr_read(p);
          v2u b = tr_read(p + 3584);
          wf[i] = v4u{a[0], a[1], b[0], b[1]};
        }
      };
      auto mm = [&](const v4u (&vf)[3], const v4u* wf, int ct0, int n) {
#pragma unroll
        for (int i = 0; i < n; ++i)
#pragma unroll
          for (int kt = 0; kt < 3; ++kt) acc[kt][ct0 + i] = mfma16(vf[kt], wf[i], acc[kt][ct0 + i]);
      };
#define SB __builtin_amdgcn_sched_barrier(0);
#define IL(n) _Pragma("unroll") for (int i_ = 0; i_ < (n); ++i_) { __builtin_amdgcn_sched_group_barrier(0x8, 1, 0); __builtin_amdgcn_sched_group_barrier(0x100, 1, 0); }
#define IL2(n) _Pragma("unroll") for (int i_ = 0; i_ < (n); ++i_) { __builtin_amdgcn_sched_group_barrier(0x8, 1, 0); __builtin_amdgcn_sched_group_barrier(0x100, 2, 0); }
      unsigned tw = 0;
      RBAR()
      rdv(va, 0, 0);
      rdw(wa, 0, 0, 4);
#pragma unroll
      for (int c = 0; c < 4; ++c) {
        rdw(wb, 2 * c, 4, 3);
        mm(va, wa, 0, 4); IL(6) SB
        RBAR()
        if (c == 1) {
          tw = __builtin_amdgcn_raw_buffer_load_b32(__builtin_amdgcn_make_buffer_rsrc((void*)(ws + (size_t)((2 * bp + bb) * 16 + h) * VW_BLK + cw * 10752), 0, 64, 0x00020000), 0, 0, 0);
          __builtin_amdgcn_sched_barrier(0);
        }
        rdv(vb, c, 1); rdw(wa, 2 * c + 1, 0, 4);
        mm(va, wb, 4, 3); IL2(6) SB
        rdw(wb, 2 * c + 1, 4, 3);
        mm(vb, wa, 0, 4); IL(6) SB
        if (c < 3) {
          RBAR()
          rdv(va, c + 1, 0);
          rdw(wa, 2 * c + 2, 0, 4);
          mm(vb, wb, 4, 3); IL2(6) SB
        } else {
          mm(vb, wb, 4, 3); SB
        }
      }
      asm volatile("" :: "v"(tw));
      const __amdgpu_buffer_rsrc_t rso = __builtin_amdgcn_make_buffer_rsrc((void*)ws, 0, 256 * VW_BLK, 0x00020000);
      {
        const int ob = ((2 * bp + bb) * 16 + h) * VW_BLK;
        const int o1 = ob + 7 * cw * 1024 + lane * 16;
        const int o2 = ob + VW_R2 + 7 * cw * 512 + lane * 8;
#pragma unroll
        for (int ct = 0; ct < 7; ++ct) {
          v4u o;
          o[0] = pack_bf16(acc[0][ct][0], acc[0][ct][1]);
          o[1] = pack_bf16(acc[0][ct][2], acc[0][ct][3]);
          o[2] = pack_bf16(acc[1][ct][0], acc[1][ct][1]);
          o[3] = pack_bf16(acc[1][ct][2], acc[1][ct][3]);
          __builtin_amdgcn_raw_buffer_store_b128(o, rso, o1 + ct * 1024, 0, 16  );
          v2u o3;
          o3[0] = pack_bf16(acc[2][ct][0], acc[2][ct][1]);
          o3[1] = pack_bf16(acc[2][ct][2], acc[2][ct][3]);
          __builtin_amdgcn_raw_buffer_store_b64(o3, rso, o2 + ct * 512, 0, 16  );
        }
      }
    }
  }
}

__global__ __launch_bounds__(512) void gqa_kernel_b(const unsigned char* __restrict__ ws, unsigned char* __restrict__ out) {
  __shared__ __attribute__((aligned(16))) float red[8 * 48 * 36];
  const int bid = blockIdx.x;
  const int xcd = bid & 7, bi = bid >> 3;
  const int b = xcd + 8 * (bi / 14), m = bi % 14;
  const int tid = threadIdx.x, lane = tid & 63;
  const int w = __builtin_amdgcn_readfirstlane(tid >> 6);
  const int lg = lane >> 4, lc = lane & 15;

  v4u p1[2][3], v1[2][2];
  v2u p2[2][3], v2[2][2];
#pragma unroll
  for (int hh = 0; hh < 2; ++hh) {
    const int h = 2 * w + hh;
    const unsigned char* pb = ws + P_OFF + (size_t)(b * 16 + h) * P_BLK;
    const unsigned char* vb = ws + (size_t)(b * 16 + h) * VW_BLK;
#pragma unroll
    for (int ct = 0; ct < 2; ++ct) {
      v1[hh][ct] = *(const v4u*)(vb + (2 * m + ct) * 1024 + lane * 16);
      v2[hh][ct] = *(const v2u*)(vb + VW_R2 + (2 * m + ct) * 512 + lane * 8);
    }
#pragma unroll
    for (int qt = 0; qt < 3; ++qt) {
      p1[hh][qt] = *(const v4u*)(pb + qt * 1024 + lane * 16);
      p2[hh][qt] = *(const v2u*)(pb + P_R2 + qt * 512 + lane * 8);
    }
  }
  __builtin_amdgcn_sched_barrier(0);
#pragma unroll
  for (int qt = 0; qt < 3; ++qt)
#pragma unroll
    for (int ct = 0; ct < 2; ++ct) {
      f32x4 a = f32x4{0.f, 0.f, 0.f, 0.f};
      a = mfma16(p1[0][qt], v1[0][ct], a);
      a = mfma16(p1[1][qt], v1[1][ct], a);
      v4u pa = {p2[0][qt][0], p2[0][qt][1], p2[1][qt][0], p2[1][qt][1]};
      v4u va = {v2[0][ct][0], v2[0][ct][1], v2[1][ct][0], v2[1][ct][1]};
      a = mfma16(pa, va, a);
#pragma unroll
      for (int r = 0; r < 4; ++r) red[(w * 48 + 16 * qt + 4 * lg + r) * 36 + 16 * ct + lc] = a[r];
    }
  __syncthreads();
  if (tid < 384) {
    const int row = tid >> 3, c4 = tid & 7;
    f32x4 s = *(const f32x4*)(red + row * 36 + 4 * c4);
#pragma unroll
    for (int wv = 1; wv < 8; ++wv) s += *(const f32x4*)(red + (wv * 48 + row) * 36 + 4 * c4);
    v2u o = {pack_bf16(s[0], s[1]), pack_bf16(s[2], s[3])};
    *(v2u*)(out + (((size_t)b * SQ + row) * DM + 32 * m + 4 * c4) * 2) = o;
  }
}

extern "C" void kernel_launch(void* const* d_in, const int* in_sizes, int n_in, void* d_out, int out_size, void* d_ws,
                              size_t ws_size, hipStream_t stream) {
  const unsigned char* x = (const unsigned char*)d_in[0];
  const unsigned char* kc = (const unsigned char*)d_in[1];
  const unsigned char* vc = (const unsigned char*)d_in[2];
  const unsigned char* wq = (const unsigned char*)d_in[3];
  const unsigned char* wo = (const unsigned char*)d_in[4];
  unsigned char* ws = (unsigned char*)d_ws;
  gqa_kernel_a<<<256, 768, 0, stream>>>(x, kc, vc, wq, wo, ws, (const unsigned char*)d_out);
  gqa_kernel_b<<<224, 512, 0, stream>>>(ws, (unsigned char*)d_out);
}
```

```cpp
#include <hip/hip_runtime.h>
#include <hip/hip_bf16.h>
#include <stdint.h>

typedef unsigned v4u __attribute__((ext_vector_type(4)));
typedef unsigned v2u __attribute__((ext_vector_type(2)));
typedef short v4s __attribute__((ext_vector_type(4)));
typedef float f32x4 __attribute__((ext_vector_type(4)));
typedef float f32x2 __attribute__((ext_vector_type(2)));
typedef __bf16 bf16x8_t __attribute__((ext_vector_type(8)));
typedef __bf16 bf16x2_t __attribute__((ext_vector_type(2)));
#define LDS_AS __attribute__((address_space(3)))

constexpr int NB = 16, SQ = 48, DM = 448, HQ = 16, HKV = 8, SK = 48, DH = 256;
constexpr int SMEM_A = 163840;
constexpr int QSLOT = 45056;
constexpr int VSLOT = 57344;
constexpr int VIMG = 49152;
constexpr int VW_BLK = 43008;
constexpr int VW_R2 = 28672;
constexpr int P_BLK = 4608;
constexpr int P_R2 = 3072;
constexpr size_t P_OFF = (size_t)256 * VW_BLK;

#define TS(i)
#define TSDUMP(WSP)
#define STAMP_BEGIN
#define STAMP_END(K, WSP)

#define AUXW 2
#define AUXX 0
#define AUXKV 2
#define AUXT 2
#define TOUCH 1
#define ROT 0
#define TWC 1
#define AUXST 16

__device__ __forceinline__ unsigned pack_bf16(float a, float b) {
  f32x2 v = {a, b};
  bf16x2_t r = __builtin_convertvector(v, bf16x2_t);
  return __builtin_bit_cast(unsigned, r);
}
__device__ __forceinline__ float round_bf16(float a) {
  __bf16 r = (__bf16)a;
  return (float)r;
}
__device__ __forceinline__ f32x4 mfma16(v4u a, v4u b, f32x4 c) {
  return __builtin_amdgcn_mfma_f32_16x16x32_bf16(__builtin_bit_cast(bf16x8_t, a), __builtin_bit_cast(bf16x8_t, b), c, 0, 0, 0);
}
__device__ __forceinline__ void swap16(float& a, float& b) { asm volatile("s_nop 1\n\tv_permlane16_swap_b32 %0, %1\n\ts_nop 1" : "+v"(a), "+v"(b)); }
__device__ __forceinline__ void swap32(float& a, float& b) { asm volatile("s_nop 1\n\tv_permlane32_swap_b32 %0, %1\n\ts_nop 1" : "+v"(a), "+v"(b)); }
__device__ __forceinline__ float xlane_max(float v) {
  float a = v, b = v;
  swap16(a, b);
  a = fmaxf(a, b); b = a;
  swap32(a, b);
  return fmaxf(a, b);
}
__device__ __forceinline__ float xlane_sum(float v) {
  float a = v, b = v;
  swap16(a, b);
  a = a + b; b = a;
  swap32(a, b);
  return a + b;
}
__device__ __forceinline__ v2u tr_read(const unsigned char* p) {
  v4s r = __builtin_amdgcn_ds_read_tr16_b64_v4i16((LDS_AS v4s*)p);
  return __builtin_bit_cast(v2u, r);
}
template <int AUX>
__device__ __forceinline__ void dma16(__amdgpu_buffer_rsrc_t rs, unsigned ldsoff, int voff, int soff) {
  if constexpr (AUX == 2)
    asm volatile("s_nop 4\n\ts_mov_b32 m0, %0\n\ts_nop 0\n\tbuffer_load_dwordx4 %1, %2, %3 offen nt lds" :: "s"(ldsoff), "v"(voff), "s"(rs), "s"(soff) : "m0", "memory");
  else
    asm volatile("s_nop 4\n\ts_mov_b32 m0, %0\n\ts_nop 0\n\tbuffer_load_dwordx4 %1, %2, %3 offen lds" :: "s"(ldsoff), "v"(voff), "s"(rs), "s"(soff) : "m0", "memory");
}
#define DMA16(RS, LDSOFF, VOFF, SOFF, AUX) dma16<AUX>((RS), lds0 + (LDSOFF), (VOFF), (SOFF))
#define WAITV(n) asm volatile("s_waitcnt vmcnt(" #n ")" ::: "memory")
#define RBAR() { asm volatile("s_waitcnt lgkmcnt(0)" ::: "memory"); __builtin_amdgcn_sched_barrier(0); __builtin_amdgcn_s_barrier(); __builtin_amdgcn_sched_barrier(0); }

constexpr int KIMG = 2 * QSLOT;
__device__ __forceinline__ int next_slot(int cur) { return cur == 0 ? 2 * QSLOT : cur - QSLOT; }

__global__ __launch_bounds__(768, 3) void gqa_kernel_a(const unsigned char* __restrict__ x, const unsigned char* __restrict__ kc,
                                                       const unsigned char* __restrict__ vc, const unsigned char* __restrict__ wq,
                                                       const unsigned char* __restrict__ wo, unsigned char* __restrict__ ws,
                                                       const unsigned char* __restrict__ outp) {
  __shared__ __attribute__((aligned(16))) unsigned char smem[SMEM_A];
  STAMP_BEGIN
  TS(0)
  const int bid = blockIdx.x;
  const int xcd = bid & 7, bi = bid >> 3;
  const int type = bi & 1, h = 2 * xcd + ((bi >> 1) & 1), bp = bi >> 2;
  const int gk = h >> 1;
  const int tid = threadIdx.x, lane = tid & 63;
  const int w = __builtin_amdgcn_readfirstlane(tid >> 6);
  const int lg = lane >> 4, lc = lane & 15;
  const int hi = lane >> 5;
  const int q4 = (lane & 15) >> 2, p4 = lane & 3;
  const int bb = (w >> 2) & 1;
  const int cq = w & 3;
  const unsigned lds0 = (unsigned)(size_t)(LDS_AS unsigned char*)smem;

  if (type == 0) {
    if (w >= 8) {
      const int ld = w - 8;
      const __amdgpu_buffer_rsrc_t rsw = __builtin_amdgcn_make_buffer_rsrc((void*)wq, 0, DM * HQ * DH * 2, 0x00020000);
      const __amdgpu_buffer_rsrc_t rsx = __builtin_amdgcn_make_buffer_rsrc((void*)(x + (size_t)bp * (2 * SQ * DM * 2)), 0, 2 * SQ * DM * 2, 0x00020000);
      const __amdgpu_buffer_rsrc_t rsk = __builtin_amdgcn_make_buffer_rsrc((void*)kc, 0, NB * HKV * SK * DH * 2, 0x00020000);
      int wv[4], xv[2];
#pragma unroll
      for (int v = 0; v < 4; ++v) {
        const int sw = (2 * (v & 1) + hi) | ((v >> 1) << 2);
        wv[v] = hi * 8192 + h * 512 + (((lane & 31) ^ (sw << 1)) * 16);
      }
#pragma unroll
      for (int v = 0; v < 2; ++v) xv[v] = (lane >> 3) * 896 + (((lane & 7) ^ ((4 * ((ld + v) & 1) + (lane >> 4)) & 7)) * 16);
      auto issue = [&](int cc, unsigned slot, int lo, int hi_) {
#pragma unroll
        for (int i = 0; i < 3; ++i) if (i >= lo && i < hi_) DMA16(rsx, slot + 32768 + (3 * ld + i) * 1024, xv[i & 1], (3 * ld + i) * 8 * 896 + 128 * cc, AUXX);
#pragma unroll
        for (int i = 0; i < 8; ++i) if (i + 3 >= lo && i + 3 < hi_)
          DMA16(rsw, slot + ld * 8192 + i * 1024, wv[(i & 1) | (((i >> 2) & 1) << 1)], (2 * cc * 32 + 16 * ld + 2 * i) * 8192, AUXW);
      };
      issue(0, 0, 0, 11);
      issue(1, 2 * QSLOT, 0, 6);
      WAITV(6); RBAR()
      issue(1, 2 * QSLOT, 6, 11);
      issue(2, QSLOT, 0, 11);
      int islot = 0;
#pragma unroll 1
      for (int c = 1; c < 5; ++c) {
        WAITV(11); RBAR()
        issue(c + 2, islot, 0, 11);
        islot = next_slot(islot);
      }
      WAITV(11);
      RBAR()
      {
#pragma unroll 1
        for (int ii = 0; ii < 12; ++ii) {
          const int i = 12 * (ld & 1) + ii;
          const int kvo = hi * 512 + (((lane & 31) ^ ((2 * i + hi) & 15)) * 16);
          DMA16(rsk, KIMG + (ld >> 1) * 24576 + i * 1024, kvo, ((2 * bp + (ld >> 1)) * HKV + gk) * (SK * DH * 2) + i * 1024, AUXKV);
        }
      }
      WAITV(12);
      RBAR()
      WAITV(0);
      RBAR()
      RBAR()
    } else {
      f32x4 acc[3][4];
#pragma unroll
      for (int qt = 0; qt < 3; ++qt)
#pragma unroll
        for (int j = 0; j < 4; ++j) acc[qt][j] = f32x4{0.f, 0.f, 0.f, 0.f};
      const int xrd = 32768 + (48 * bb + lc) * 128 + ((lg ^ ((lc >> 1) & 7)) * 16);
      const int srd = q4 | ((lg & 1) << 2);
      const int wrd = (8 * lg + q4) * 512 + (((8 * cq + (p4 >> 1)) ^ (srd << 1)) * 16) + (p4 & 1) * 8;
      int cslot = 0;
#define ILQ(n) _Pragma("unroll") for (int i_ = 0; i_ < (n); ++i_) { __builtin_amdgcn_sched_group_barrier(0x8, 1, 0); __builtin_amdgcn_sched_group_barrier(0x100, 1, 0); }
      v4u xa[3], xb2[3], wa[4], wb[4], ka[3][2];
      auto rd = [&](v4u (&xf)[3], v4u (&wf)[4], const unsigned char* slot, int ss) {
#pragma unroll
        for (int qt = 0; qt < 3; ++qt) xf[qt] = *(const v4u*)(slot + ((xrd ^ (64 * ss)) + qt * 2048));
#pragma unroll
        for (int j = 0; j < 4; ++j) {
          v2u a = tr_read(slot + ss * 16384 + (wrd ^ (32 * j)));
          v2u b = tr_read(slot + ss * 16384 + (wrd ^ (32 * j)) + 2048);
          wf[j] = v4u{a[0], a[1], b[0], b[1]};
        }
      };
      auto mm = [&](const v4u (&xf)[3], const v4u (&wf)[4]) {
#pragma unroll
        for (int j = 0; j < 4; ++j)
#pragma unroll
          for (int qt = 0; qt < 3; ++qt) acc[qt][j] = mfma16(wf[j], xf[qt], acc[qt][j]);
      };
      unsigned tw = 0;
      TS(1)
      RBAR()
      rd(xa, wa, smem, 0);
#pragma unroll
      for (int c = 0; c < 7; ++c) {
        const unsigned char* slot = smem + cslot;
        rd(xb2, wb, slot, 1);
        mm(xa, wa);
        ILQ(11)
        __builtin_amdgcn_sched_barrier(0);
        cslot = next_slot(cslot);
        RBAR()
        if (c == TWC) {
          const unsigned char* ta;
          if (w < 2)      ta = kc + (size_t)((2 * bp + w) * HKV + gk) * (SK * DH * 2);
          else if (w < 4) ta = ws + P_OFF + (size_t)((2 * bp + (w - 2)) * 16 + h) * P_BLK;
          else            ta = outp + (size_t)(xcd + 8 * (w & 1)) * (SQ * DM * 2) + (bi & 15) * 2688;
          tw = __builtin_amdgcn_raw_buffer_load_b32(__builtin_amdgcn_make_buffer_rsrc((void*)ta, 0, 64, 0x00020000), 0, 0, 0);
          __builtin_amdgcn_sched_barrier(0);
        }
        if (c < 6) rd(xa, wa, smem + cslot, 0);
        else {
          const unsigned char* kl = smem + KIMG + (48 * bb + lc) * 512 + (lg & 1) * 8;
#pragma unroll
          for (int kt = 0; kt < 3; ++kt)
#pragma unroll
            for (int f = 0; f < 2; ++f) {
              v2u a = *(const v2u*)(kl + kt * 8192 + (((8 * cq + 4 * f + (lg >> 1)) ^ lc) * 16));
              v2u b = *(const v2u*)(kl + kt * 8192 + (((8 * cq + 4 * f + 2 + (lg >> 1)) ^ lc) * 16));
              ka[kt][f] = v4u{a[0], a[1], b[0], b[1]};
            }
        }
        mm(xb2, wb);
        ILQ(11)
        __builtin_amdgcn_sched_barrier(0);
      }
      f32x4 sacc[3][3];
      {
#pragma unroll
        for (int qt = 0; qt < 3; ++qt) {
#pragma unroll
          for (int kt = 0; kt < 3; ++kt) sacc[kt][qt] = f32x4{0.f, 0.f, 0.f, 0.f};
#pragma unroll
          for (int f = 0; f < 2; ++f) {
            v4u qf;
            qf[0] = pack_bf16(acc[qt][2 * f][0], acc[qt][2 * f][1]);
            qf[1] = pack_bf16(acc[qt][2 * f][2], acc[qt][2 * f][3]);
            qf[2] = pack_bf16(acc[qt][2 * f + 1][0], acc[qt][2 * f + 1][1]);
            qf[3] = pack_bf16(acc[qt][2 * f + 1][2], acc[qt][2 * f + 1][3]);
#pragma unroll
            for (int kt = 0; kt < 3; ++kt) sacc[kt][qt] = mfma16(ka[kt][f], qf, sacc[kt][qt]);
          }
        }
      }
      asm volatile("" :: "v"(tw));
      TS(9)
      unsigned char* sp = smem;
#pragma unroll
      for (int kt = 0; kt < 3; ++kt)
#pragma unroll
        for (int qt = 0; qt < 3; ++qt) *(f32x4*)(sp + ((w * 9 + qt * 3 + kt) * 64 + lane) * 16) = sacc[kt][qt];
      TS(10)
      RBAR()
      TS(11)
      if (w < 6) {
        const int b2 = w >= 3 ? 1 : 0, qt = w - 3 * b2;
        f32x4 pr[4][3];
        const unsigned char* spl = sp + ((36 * b2 + qt * 3) * 64 + lane) * 16;
#pragma unroll
        for (int wv = 0; wv < 4; ++wv)
#pragma unroll
          for (int kt = 0; kt < 3; ++kt) pr[wv][kt] = *(const f32x4*)(spl + (wv * 9 + kt) * 1024);
        __builtin_amdgcn_sched_barrier(0);
        float sv[12];
#pragma unroll
        for (int kt = 0; kt < 3; ++kt) {
          const f32x4 a = (pr[0][kt] + pr[1][kt]) + (pr[2][kt] + pr[3][kt]);
          const unsigned u0 = pack_bf16(a[0], a[1]), u1 = pack_bf16(a[2], a[3]);
          sv[4 * kt + 0] = __builtin_bit_cast(float, u0 << 16);
          sv[4 * kt + 1] = __builtin_bit_cast(float, u0 & 0xffff0000u);
          sv[4 * kt + 2] = __builtin_bit_cast(float, u1 << 16);
          sv[4 * kt + 3] = __builtin_bit_cast(float, u1 & 0xffff0000u);
        }
        float m = fmaxf(fmaxf(fmaxf(sv[0], sv[1]), fmaxf(sv[2], sv[3])), fmaxf(fmaxf(sv[4], sv[5]), fmaxf(sv[6], sv[7])));
        m = fmaxf(m, fmaxf(fmaxf(sv[8], sv[9]), fmaxf(sv[10], sv[11])));
        m = xlane_max(m);
        const float cs = 0.0625f * 1.44269504088896340736f;
        const float mc = -m * cs;
        float sum = 0.f;
#pragma unroll
        for (int i = 0; i < 12; ++i) {
          sv[i] = __builtin_amdgcn_exp2f(__builtin_fmaf(sv[i], cs, mc));
          sum += sv[i];
        }
        sum = xlane_sum(sum);
        const float inv = __builtin_amdgcn_rcpf(sum);
        v4u p1;
        v2u p2;
        p1[0] = pack_bf16(sv[0] * inv, sv[1] * inv);
        p1[1] = pack_bf16(sv[2] * inv, sv[3] * inv);
        p1[2] = pack_bf16(sv[4] * inv, sv[5] * inv);
        p1[3] = pack_bf16(sv[6] * inv, sv[7] * inv);
        p2[0] = pack_bf16(sv[8] * inv, sv[9] * inv);
        p2[1] = pack_bf16(sv[10] * inv, sv[11] * inv);
        unsigned char* pb = ws + P_OFF + (size_t)((2 * bp + b2) * 16 + h) * P_BLK;
        *(v4u*)(pb + qt * 1024 + lane * 16) = p1;
        *(v2u*)(pb + P_R2 + qt * 512 + lane * 8) = p2;
      }
    }
  } else {
    if (w >= 8) {
      const int ld = w - 8;
      const __amdgpu_buffer_rsrc_t rs = __builtin_amdgcn_make_buffer_rsrc((void*)wo, 0, DM * HQ * DH * 2, 0x00020000);
      const __amdgpu_buffer_rsrc_t rsv = __builtin_amdgcn_make_buffer_rsrc((void*)vc, 0, NB * HKV * SK * DH * 2, 0x00020000);
      int vv[2];
#pragma unroll
      for (int v = 0; v < 2; ++v) vv[v] = (lane >> 3) * 512 + (((lane & 7) ^ ((4 * ((ld + v) & 1) + (lane >> 4)) & 7)) * 16);
      int wo_voff[7];
#pragma unroll
      for (int r = 0; r < 7; ++r) {
        const int P = 1024 * r + 16 * lane;
        const int row = P / 896;
        const int phys = (P - 896 * row) >> 4;
        const int sp_ = ((row >> 1) & 1) | ((ld & 1) << 1);
        wo_voff[r] = row * 896 + ((phys ^ (sp_ << 1)) * 16);
      }
      auto issue = [&](int st) {
        if ((st & 1) == 0) {
#pragma unroll
          for (int i = 0; i < 3; ++i)
            DMA16(rsv, (st >> 1) * 12288 + (3 * ld + i) * 1024, vv[i & 1],
                  ((2 * bp + (ld >> 1)) * HKV + gk) * (SK * DH * 2) + (3 * (ld & 1) + i) * 8 * 512 + 64 * st, AUXKV);
        }
        const unsigned slot = VIMG + (st & 3) * 28672 + ld * 7168;
#pragma unroll
        for (int r = 0; r < 7; ++r) DMA16(rs, slot + r * 1024, wo_voff[r], (h * 256 + 32 * st + 8 * ld) * 896, AUXW);
      };
      issue(0); issue(1);
      WAITV(7);  RBAR() issue(2); issue(3);
      WAITV(17); RBAR() issue(4);
      WAITV(17); RBAR() issue(5);
      WAITV(17); RBAR() issue(6);
      WAITV(17); RBAR() issue(7);
      WAITV(17); RBAR()
      WAITV(7);  RBAR()
      WAITV(0);  RBAR()
    } else {
      const int cw = cq;
      f32x4 acc[3][7];
#pragma unroll
      for (int kt = 0; kt < 3; ++kt)
#pragma unroll
        for (int ct = 0; ct < 7; ++ct) acc[kt][ct] = f32x4{0.f, 0.f, 0.f, 0.f};
      const int vrd = (48 * bb + lc) * 128 + ((lg ^ ((lc >> 1) & 7)) * 16);
      const int sp2 = ((q4 >> 1) & 1) | ((lg & 1) << 1);
      const int wrow = (8 * lg + q4) * 896 + (p4 & 1) * 8;
      v4u va[3], vb[3], wa[4], wb[3];
      auto rdv = [&](v4u (&vf)[3], int c, int ss) {
#pragma unroll
        for (int kt = 0; kt < 3; ++kt) vf[kt] = *(const v4u*)(smem + c * 12288 + ((vrd ^ (64 * ss)) + kt * 2048));
      };
      int wbase[4];
#pragma unroll
      for (int i = 0; i < 4; ++i) wbase[i] = VIMG + wrow + (((14 * cw + 2 * i + (p4 >> 1)) ^ (sp2 << 1)) * 16);
      auto rdw = [&](v4u* wf, int st, int ct0, int n) {
#pragma unroll
        for (int i = 0; i < n; ++i) {
          const unsigned char* p = smem + wbase[i] + ((st & 3) * 28672 + (ct0 ? 128 : 0));
          v2u a = tr_read(p);
          v2u b = tr_read(p + 3584);
          wf[i] = v4u{a[0], a[1], b[0], b[1]};
        }
      };
      auto mm = [&](const v4u (&vf)[3], const v4u* wf, int ct0, int n) {
#pragma unroll
        for (int i = 0; i < n; ++i)
#pragma unroll
          for (int kt = 0; kt < 3; ++kt) acc[kt][ct0 + i] = mfma16(vf[kt], wf[i], acc[kt][ct0 + i]);
      };
#define SB __builtin_amdgcn_sched_barrier(0);
#define IL(n) _Pragma("unroll") for (int i_ = 0; i_ < (n); ++i_) { __builtin_amdgcn_sched_group_barrier(0x8, 1, 0); __builtin_amdgcn_sched_group_barrier(0x100, 1, 0); }
#define IL2(n) _Pragma("unroll") for (int i_ = 0; i_ < (n); ++i_) { __builtin_amdgcn_sched_group_barrier(0x8, 1, 0); __builtin_amdgcn_sched_group_barrier(0x100, 2, 0); }
      unsigned tw = 0;
      TS(1)
      RBAR()
      rdv(va, 0, 0);
      rdw(wa, 0, 0, 4);
#pragma unroll
      for (int c = 0; c < 4; ++c) {
        rdw(wb, 2 * c, 4, 3);
        mm(va, wa, 0, 4); IL(6) SB
        RBAR()
        if (c == TWC) {
          tw = __builtin_amdgcn_raw_buffer_load_b32(__builtin_amdgcn_make_buffer_rsrc((void*)(ws + (size_t)((2 * bp + bb) * 16 + h) * VW_BLK + cw * 10752), 0, 64, 0x00020000), 0, 0, 0);
          __builtin_amdgcn_sched_barrier(0);
        }
        rdv(vb, c, 1); rdw(wa, 2 * c + 1, 0, 4);
        mm(va, wb, 4, 3); IL2(6) SB
        rdw(wb, 2 * c + 1, 4, 3);
        mm(vb, wa, 0, 4); IL(6) SB
        if (c < 3) {
          RBAR()
          rdv(va, c + 1, 0);
          rdw(wa, 2 * c + 2, 0, 4);
          mm(vb, wb, 4, 3); IL2(6) SB
        } else {
          mm(vb, wb, 4, 3); SB
        }
      }
      asm volatile("" :: "v"(tw));
      const __amdgpu_buffer_rsrc_t rso = __builtin_amdgcn_make_buffer_rsrc((void*)ws, 0, 256 * VW_BLK, 0x00020000);
      {
        const int ob = ((2 * bp + bb) * 16 + h) * VW_BLK;
        const int o1 = ob + 7 * cw * 1024 + lane * 16;
        const int o2 = ob + VW_R2 + 7 * cw * 512 + lane * 8;
#pragma unroll
        for (int ct = 0; ct < 7; ++ct) {
          v4u o;
          o[0] = pack_bf16(acc[0][ct][0], acc[0][ct][1]);
          o[1] = pack_bf16(acc[0][ct][2], acc[0][ct][3]);
          o[2] = pack_bf16(acc[1][ct][0], acc[1][ct][1]);
          o[3] = pack_bf16(acc[1][ct][2], acc[1][ct][3]);
          __builtin_amdgcn_raw_buffer_store_b128(o, rso, o1 + ct * 1024, 0, AUXST);
          v2u o3;
          o3[0] = pack_bf16(acc[2][ct][0], acc[2][ct][1]);
          o3[1] = pack_bf16(acc[2][ct][2], acc[2][ct][3]);
          __builtin_amdgcn_raw_buffer_store_b64(o3, rso, o2 + ct * 512, 0, AUXST);
        }
      }
      TS(10)
    }
  }
  TS(12)
  TSDUMP(ws)
  STAMP_END(0, ws)
}

__global__ __launch_bounds__(512) void gqa_kernel_b(const unsigned char* __restrict__ ws, unsigned char* __restrict__ out) {
  __shared__ __attribute__((aligned(16))) float red[8 * 48 * 36];
  STAMP_BEGIN
  const int bid = blockIdx.x;
  const int xcd = bid & 7, bi = bid >> 3;
  const int b = xcd + 8 * (bi / 14), m = bi % 14;
  const int tid = threadIdx.x, lane = tid & 63;
  const int w = __builtin_amdgcn_readfirstlane(tid >> 6);
  const int lg = lane >> 4, lc = lane & 15;

  v4u p1[2][3], v1[2][2];
  v2u p2[2][3], v2[2][2];
#pragma unroll
  for (int hh = 0; hh < 2; ++hh) {
    const int h = 2 * w + hh;
    const unsigned char* pb = ws + P_OFF + (size_t)(b * 16 + h) * P_BLK;
    const unsigned char* vb = ws + (size_t)(b * 16 + h) * VW_BLK;
#pragma unroll
    for (int ct = 0; ct < 2; ++ct) {
      v1[hh][ct] = *(const v4u*)(vb + (2 * m + ct) * 1024 + lane * 16);
      v2[hh][ct] = *(const v2u*)(vb + VW_R2 + (2 * m + ct) * 512 + lane * 8);
    }
#pragma unroll
    for (int qt = 0; qt < 3; ++qt) {
      p1[hh][qt] = *(const v4u*)(pb + qt * 1024 + lane * 16);
      p2[hh][qt] = *(const v2u*)(pb + P_R2 + qt * 512 + lane * 8);
    }
  }
  __builtin_amdgcn_sched_barrier(0);
#pragma unroll
  for (int qt = 0; qt < 3; ++qt)
#pragma unroll
    for (int ct = 0; ct < 2; ++ct) {
      f32x4 a = f32x4{0.f, 0.f, 0.f, 0.f};
      a = mfma16(p1[0][qt], v1[0][ct], a);
      a = mfma16(p1[1][qt], v1[1][ct], a);
      v4u pa = {p2[0][qt][0], p2[0][qt][1], p2[1][qt][0], p2[1][qt][1]};
      v4u va = {v2[0][ct][0], v2[0][ct][1], v2[1][ct][0], v2[1][ct][1]};
      a = mfma16(pa, va, a);
#pragma unroll
      for (int r = 0; r < 4; ++r) red[(w * 48 + 16 * qt + 4 * lg + r) * 36 + 16 * ct + lc] = a[r];
    }
  __syncthreads();
  if (tid < 384) {
    const int row = tid >> 3, c4 = tid & 7;
    f32x4 s = *(const f32x4*)(red + row * 36 + 4 * c4);
#pragma unroll
    for (int wv = 1; wv < 8; ++wv) s += *(const f32x4*)(red + (wv * 48 + row) * 36 + 4 * c4);
    v2u o = {pack_bf16(s[0], s[1]), pack_bf16(s[2], s[3])};
    *(v2u*)(out + (((size_t)b * SQ + row) * DM + 32 * m + 4 * c4) * 2) = o;
  }
  STAMP_END(1, ws)
}

extern "C" void kernel_launch(void* const* d_in, const int* in_sizes, int n_in, void* d_out, int out_size, void* d_ws,
                              size_t ws_size, hipStream_t stream) {
  const unsigned char* x = (const unsigned char*)d_in[0];
  const unsigned char* kc = (const unsigned char*)d_in[1];
  const unsigned char* vc = (const unsigned char*)d_in[2];
  const unsigned char* wq = (const unsigned char*)d_in[3];
  const unsigned char* wo = (const unsigned char*)d_in[4];
  unsigned char* ws = (unsigned char*)d_ws;
  gqa_kernel_a<<<256, 768, 0, stream>>>(x, kc, vc, wq, wo, ws, (const unsigned char*)d_out);
  gqa_kernel_b<<<224, 512, 0, stream>>>(ws, (unsigned char*)d_out);
}
```

```cpp
#include <hip/hip_runtime.h>
#include <hip/hip_bf16.h>
#include <stdint.h>

typedef unsigned v4u __attribute__((ext_vector_type(4)));
typedef unsigned v2u __attribute__((ext_vector_type(2)));
typedef short v4s __attribute__((ext_vector_type(4)));
typedef float f32x4 __attribute__((ext_vector_type(4)));
typedef float f32x2 __attribute__((ext_vector_type(2)));
typedef __bf16 bf16x8_t __attribute__((ext_vector_type(8)));
typedef __bf16 bf16x2_t __attribute__((ext_vector_type(2)));
#define LDS_AS __attribute__((address_space(3)))

constexpr int NB = 16, SQ = 48, DM = 448, HQ = 16, HKV = 8, SK = 48, DH = 256;
constexpr int SMEM_A = 163840;
constexpr int QSLOT = 45056;
constexpr int VIMG = 49152;
constexpr int VW_BLK = 43008;
constexpr int VW_R2 = 28672;
constexpr int P_BLK = 4608;
constexpr int P_R2 = 3072;
constexpr size_t P_OFF = (size_t)256 * VW_BLK;

__device__ __forceinline__ unsigned pack_bf16(float a, float b) {
  f32x2 v = {a, b};
  bf16x2_t r = __builtin_convertvector(v, bf16x2_t);
  return __builtin_bit_cast(unsigned, r);
}
__device__ __forceinline__ f32x4 mfma16(v4u a, v4u b, f32x4 c) {
  return __builtin_amdgcn_mfma_f32_16x16x32_bf16(__builtin_bit_cast(bf16x8_t, a), __builtin_bit_cast(bf16x8_t, b), c, 0, 0, 0);
}
__device__ __forceinline__ void swap16(float& a, float& b) { asm volatile("s_nop 1\n\tv_permlane16_swap_b32 %0, %1\n\ts_nop 1" : "+v"(a), "+v"(b)); }
__device__ __forceinline__ void swap32(float& a, float& b) { asm volatile("s_nop 1\n\tv_permlane32_swap_b32 %0, %1\n\ts_nop 1" : "+v"(a), "+v"(b)); }
__device__ __forceinline__ float xlane_max(float v) {
  float a = v, b = v;
  swap16(a, b);
  a = fmaxf(a, b); b = a;
  swap32(a, b);
  return fmaxf(a, b);
}
__device__ __forceinline__ float xlane_sum(float v) {
  float a = v, b = v;
  swap16(a, b);
  a = a + b; b = a;
  swap32(a, b);
  return a + b;
}
__device__ __forceinline__ v2u tr_read(const unsigned char* p) {
  v4s r = __builtin_amdgcn_ds_read_tr16_b64_v4i16((LDS_AS v4s*)p);
  return __builtin_bit_cast(v2u, r);
}
__device__ __forceinline__ void dma16(__amdgpu_buffer_rsrc_t rs, unsigned ldsoff, int voff, int soff) {
  asm volatile("s_nop 4\n\ts_mov_b32 m0, %0\n\ts_nop 0\n\tbuffer_load_dwordx4 %1, %2, %3 offen lds" :: "s"(ldsoff), "v"(voff), "s"(rs), "s"(soff) : "m0", "memory");
}
#define DMA16(RS, LDSOFF, VOFF, SOFF) dma16((RS), lds0 + (LDSOFF), (VOFF), (SOFF))
#define WAITV(n) asm volatile("s_waitcnt vmcnt(" #n ")" ::: "memory")
#define RBAR() { asm volatile("s_waitcnt lgkmcnt(0)" ::: "memory"); __builtin_amdgcn_sched_barrier(0); __builtin_amdgcn_s_barrier(); __builtin_amdgcn_sched_barrier(0); }

constexpr int KIMG = 2 * QSLOT;
__device__ __forceinline__ int next_slot(int cur) { return cur == 0 ? 2 * QSLOT : cur - QSLOT; }

__global__ __launch_bounds__(768, 3) void gqa_kernel_a(const unsigned char* __restrict__ x, const unsigned char* __restrict__ kc,
                                                       const unsigned char* __restrict__ vc, const unsigned char* __restrict__ wq,
                                                       const unsigned char* __restrict__ wo, unsigned char* __restrict__ ws,
                                                       const unsigned char* __restrict__ outp) {
  __shared__ __attribute__((aligned(16))) unsigned char smem[SMEM_A];
  const int bid = blockIdx.x;
  const int xcd = bid & 7, bi = bid >> 3;
  const int type = bi & 1, h = 2 * xcd + ((bi >> 1) & 1), bp = bi >> 2;
  const int gk = h >> 1;
  const int tid = threadIdx.x, lane = tid & 63;
  const int w = __builtin_amdgcn_readfirstlane(tid >> 6);
  const int lg = lane >> 4, lc = lane & 15;
  const int hi = lane >> 5;
  const int q4 = (lane & 15) >> 2, p4 = lane & 3;
  const int bb = (w >> 2) & 1;
  const int cq = w & 3;
  const unsigned lds0 = (unsigned)(size_t)(LDS_AS unsigned char*)smem;

  if (type == 0) {
    if (w >= 8) {
      const int ld = w - 8;
      const __amdgpu_buffer_rsrc_t rsw = __builtin_amdgcn_make_buffer_rsrc((void*)wq, 0, DM * HQ * DH * 2, 0x00020000);
      const __amdgpu_buffer_rsrc_t rsx = __builtin_amdgcn_make_buffer_rsrc((void*)(x + (size_t)bp * (2 * SQ * DM * 2)), 0, 2 * SQ * DM * 2, 0x00020000);
      const __amdgpu_buffer_rsrc_t rsk = __builtin_amdgcn_make_buffer_rsrc((void*)kc, 0, NB * HKV * SK * DH * 2, 0x00020000);
      int wv[4], xv[2];
#pragma unroll
      for (int v = 0; v < 4; ++v) {
        const int sw = (2 * (v & 1) + hi) | ((v >> 1) << 2);
        wv[v] = hi * 8192 + h * 512 + (((lane & 31) ^ (sw << 1)) * 16);
      }
#pragma unroll
      for (int v = 0; v < 2; ++v) xv[v] = (lane >> 3) * 896 + (((lane & 7) ^ ((4 * ((ld + v) & 1) + (lane >> 4)) & 7)) * 16);
      auto issue = [&](int cc, unsigned slot, int lo, int hi_) {
#pragma unroll
        for (int i = 0; i < 3; ++i) if (i >= lo && i < hi_) DMA16(rsx, slot + 32768 + (3 * ld + i) * 1024, xv[i & 1], (3 * ld + i) * 8 * 896 + 128 * cc);
#pragma unroll
        for (int i = 0; i < 8; ++i) if (i + 3 >= lo && i + 3 < hi_)
          DMA16(rsw, slot + ld * 8192 + i * 1024, wv[(i & 1) | (((i >> 2) & 1) << 1)], (2 * cc * 32 + 16 * ld + 2 * i) * 8192);
      };
      issue(0, 0, 0, 11);
      issue(1, 2 * QSLOT, 0, 6);
      WAITV(6); RBAR()
      issue(1, 2 * QSLOT, 6, 11);
      issue(2, QSLOT, 0, 11);
      int islot = 0;
#pragma unroll 1
      for (int c = 1; c < 5; ++c) {
        WAITV(11); RBAR()
        issue(c + 2, islot, 0, 11);
        islot = next_slot(islot);
      }
      WAITV(11);
      RBAR()
      {
#pragma unroll 1
        for (int ii = 0; ii < 12; ++ii) {
          const int i = 12 * (ld & 1) + ii;
          const int kvo = hi * 512 + (((lane & 31) ^ ((2 * i + hi) & 15)) * 16);
          DMA16(rsk, KIMG + (ld >> 1) * 24576 + i * 1024, kvo, ((2 * bp + (ld >> 1)) * HKV + gk) * (SK * DH * 2) + i * 1024);
        }
      }
      WAITV(12);
      RBAR()
      WAITV(0);
      RBAR()
      RBAR()
    } else {
      f32x4 acc[3][4];
#pragma unroll
      for (int qt = 0; qt < 3; ++qt)
#pragma unroll
        for (int j = 0; j < 4; ++j) acc[qt][j] = f32x4{0.f, 0.f, 0.f, 0.f};
      const int xrd = 32768 + (48 * bb + lc) * 128 + ((lg ^ ((lc >> 1) & 7)) * 16);
      const int srd = q4 | ((lg & 1) << 2);
      const int wrd = (8 * lg + q4) * 512 + (((8 * cq + (p4 >> 1)) ^ (srd << 1)) * 16) + (p4 & 1) * 8;
      int cslot = 0;
#define ILQ(n) _Pragma("unroll") for (int i_ = 0; i_ < (n); ++i_) { __builtin_amdgcn_sched_group_barrier(0x8, 1, 0); __builtin_amdgcn_sched_group_barrier(0x100, 1, 0); }
      v4u xa[3], xb2[3], wa[4], wb[4], ka[3][2];
      auto rd = [&](v4u (&xf)[3], v4u (&wf)[4], const unsigned char* slot, int ss) {
#pragma unroll
        for (int qt = 0; qt < 3; ++qt) xf[qt] = *(const v4u*)(slot + ((xrd ^ (64 * ss)) + qt * 2048));
#pragma unroll
        for (int j = 0; j < 4; ++j) {
          v2u a = tr_read(slot + ss * 16384 + (wrd ^ (32 * j)));
          v2u b = tr_read(slot + ss * 16384 + (wrd ^ (32 * j)) + 2048);
          wf[j] = v4u{a[0], a[1], b[0], b[1]};
        }
      };
      auto mm = [&](const v4u (&xf)[3], const v4u (&wf)[4]) {
#pragma unroll
        for (int j = 0; j < 4; ++j)
#pragma unroll
          for (int qt = 0; qt < 3; ++qt) acc[qt][j] = mfma16(wf[j], xf[qt], acc[qt][j]);
      };
      unsigned tw = 0;
      RBAR()
      rd(xa, wa, smem, 0);
#pragma unroll
      for (int c = 0; c < 7; ++c) {
        const unsigned char* slot = smem + cslot;
        rd(xb2, wb, slot, 1);
        mm(xa, wa);
        ILQ(11)
        __builtin_amdgcn_sched_barrier(0);
        cslot = next_slot(cslot);
        RBAR()
        if (c == 0) {
          const unsigned char* ta;
          int toff = 0, tlen = 64;
          if (w < 2)      ta = kc + (size_t)((2 * bp + w) * HKV + gk) * (SK * DH * 2);
          else if (w < 4) ta = ws + P_OFF + (size_t)((2 * bp + (w - 2)) * 16 + h) * P_BLK;
          else if (w < 6) ta = outp + (size_t)(xcd + 8 * (w & 1)) * (SQ * DM * 2) + (bi & 15) * 2688;
          else { ta = wq; tlen = DM * HQ * DH * 2; const int r = ((w - 6) * 64 + lane) * 8; toff = (r < DM && (w - 6) * 64 + lane < 56) ? r * 8192 + h * 512 : 0x7ffffff0; }
          tw = __builtin_amdgcn_raw_buffer_load_b32(__builtin_amdgcn_make_buffer_rsrc((void*)ta, 0, tlen, 0x00020000), toff, 0, 0);
          __builtin_amdgcn_sched_barrier(0);
        }
        if (c < 6) rd(xa, wa, smem + cslot, 0);
        else {
          const unsigned char* kl = smem + KIMG + (48 * bb + lc) * 512 + (lg & 1) * 8;
#pragma unroll
          for (int kt = 0; kt < 3; ++kt)
#pragma unroll
            for (int f = 0; f < 2; ++f) {
              v2u a = *(const v2u*)(kl + kt * 8192 + (((8 * cq + 4 * f + (lg >> 1)) ^ lc) * 16));
              v2u b = *(const v2u*)(kl + kt * 8192 + (((8 * cq + 4 * f + 2 + (lg >> 1)) ^ lc) * 16));
              ka[kt][f] = v4u{a[0], a[1], b[0], b[1]};
            }
        }
        mm(xb2, wb);
        ILQ(11)
        __builtin_amdgcn_sched_barrier(0);
      }
      f32x4 sacc[3][3];
      {
#pragma unroll
        for (int qt = 0; qt < 3; ++qt) {
#pragma unroll
          for (int kt = 0; kt < 3; ++kt) sacc[kt][qt] = f32x4{0.f, 0.f, 0.f, 0.f};
#pragma unroll
          for (int f = 0; f < 2; ++f) {
            v4u qf;
            qf[0] = pack_bf16(acc[qt][2 * f][0], acc[qt][2 * f][1]);
            qf[1] = pack_bf16(acc[qt][2 * f][2], acc[qt][2 * f][3]);
            qf[2] = pack_bf16(acc[qt][2 * f + 1][0], acc[qt][2 * f + 1][1]);
            qf[3] = pack_bf16(acc[qt][2 * f + 1][2], acc[qt][2 * f + 1][3]);
#pragma unroll
            for (int kt = 0; kt < 3; ++kt) sacc[kt][qt] = mfma16(ka[kt][f], qf, sacc[kt][qt]);
          }
        }
      }
      asm volatile("" :: "v"(tw));
      unsigned char* sp = smem;
#pragma unroll
      for (int kt = 0; kt < 3; ++kt)
#pragma unroll
        for (int qt = 0; qt < 3; ++qt) *(f32x4*)(sp + ((w * 9 + qt * 3 + kt) * 64 + lane) * 16) = sacc[kt][qt];
      RBAR()
      if (w < 6) {
        const int b2 = w >= 3 ? 1 : 0, qt = w - 3 * b2;
        f32x4 pr[4][3];
        const unsigned char* spl = sp + ((36 * b2 + qt * 3) * 64 + lane) * 16;
#pragma unroll
        for (int wv = 0; wv < 4; ++wv)
#pragma unroll
          for (int kt = 0; kt < 3; ++kt) pr[wv][kt] = *(const f32x4*)(spl + (wv * 9 + kt) * 1024);
        __builtin_amdgcn_sched_barrier(0);
        float sv[12];
#pragma unroll
        for (int kt = 0; kt < 3; ++kt) {
          const f32x4 a = (pr[0][kt] + pr[1][kt]) + (pr[2][kt] + pr[3][kt]);
          const unsigned u0 = pack_bf16(a[0], a[1]), u1 = pack_bf16(a[2], a[3]);
          sv[4 * kt + 0] = __builtin_bit_cast(float, u0 << 16);
          sv[4 * kt + 1] = __builtin_bit_cast(float, u0 & 0xffff0000u);
          sv[4 * kt + 2] = __builtin_bit_cast(float, u1 << 16);
          sv[4 * kt + 3] = __builtin_bit_cast(float, u1 & 0xffff0000u);
        }
        float m = fmaxf(fmaxf(fmaxf(sv[0], sv[1]), fmaxf(sv[2], sv[3])), fmaxf(fmaxf(sv[4], sv[5]), fmaxf(sv[6], sv[7])));
        m = fmaxf(m, fmaxf(fmaxf(sv[8], sv[9]), fmaxf(sv[10], sv[11])));
        m = xlane_max(m);
        const float cs = 0.0625f * 1.44269504088896340736f;
        const float mc = -m * cs;
        float sum = 0.f;
#pragma unroll
        for (int i = 0; i < 12; ++i) {
          sv[i] = __builtin_amdgcn_exp2f(__builtin_fmaf(sv[i], cs, mc));
          sum += sv[i];
        }
        sum = xlane_sum(sum);
        const float inv = __builtin_amdgcn_rcpf(sum);
        v4u p1;
        v2u p2;
        p1[0] = pack_bf16(sv[0] * inv, sv[1] * inv);
        p1[1] = pack_bf16(sv[2] * inv, sv[3] * inv);
        p1[2] = pack_bf16(sv[4] * inv, sv[5] * inv);
        p1[3] = pack_bf16(sv[6] * inv, sv[7] * inv);
        p2[0] = pack_bf16(sv[8] * inv, sv[9] * inv);
        p2[1] = pack_bf16(sv[10] * inv, sv[11] * inv);
        unsigned char* pb = ws + P_OFF + (size_t)((2 * bp + b2) * 16 + h) * P_BLK;
        *(v4u*)(pb + qt * 1024 + lane * 16) = p1;
        *(v2u*)(pb + P_R2 + qt * 512 + lane * 8) = p2;
      }
    }
  } else {
    if (w >= 8) {
      const int ld = w - 8;
      const __amdgpu_buffer_rsrc_t rs = __builtin_amdgcn_make_buffer_rsrc((void*)wo, 0, DM * HQ * DH * 2, 0x00020000);
      const __amdgpu_buffer_rsrc_t rsv = __builtin_amdgcn_make_buffer_rsrc((void*)vc, 0, NB * HKV * SK * DH * 2, 0x00020000);
      int vv[2];
#pragma unroll
      for (int v = 0; v < 2; ++v) vv[v] = (lane >> 3) * 512 + (((lane & 7) ^ ((4 * ((ld + v) & 1) + (lane >> 4)) & 7)) * 16);
      int wo_voff[7];
#pragma unroll
      for (int r = 0; r < 7; ++r) {
        const int P = 1024 * r + 16 * lane;
        const int row = P / 896;
        const int phys = (P - 896 * row) >> 4;
        const int sp_ = ((row >> 1) & 1) | ((ld & 1) << 1);
        wo_voff[r] = row * 896 + ((phys ^ (sp_ << 1)) * 16);
      }
      auto issue = [&](int st) {
        if ((st & 1) == 0) {
#pragma unroll
          for (int i = 0; i < 3; ++i)
            DMA16(rsv, (st >> 1) * 12288 + (3 * ld + i) * 1024, vv[i & 1],
                  ((2 * bp + (ld >> 1)) * HKV + gk) * (SK * DH * 2) + (3 * (ld & 1) + i) * 8 * 512 + 64 * st);
        }
        const unsigned slot = VIMG + (st & 3) * 28672 + ld * 7168;
#pragma unroll
        for (int r = 0; r < 7; ++r) DMA16(rs, slot + r * 1024, wo_voff[r], (h * 256 + 32 * st + 8 * ld) * 896);
      };
      issue(0); issue(1);
      WAITV(7);  RBAR() issue(2); issue(3);
      WAITV(17); RBAR() issue(4);
      WAITV(17); RBAR() issue(5);
      WAITV(17); RBAR() issue(6);
      WAITV(17); RBAR() issue(7);
      WAITV(17); RBAR()
      WAITV(7);  RBAR()
      WAITV(0);  RBAR()
    } else {
      const int cw = cq;
      f32x4 acc[3][7];
#pragma unroll
      for (int kt = 0; kt < 3; ++kt)
#pragma unroll
        for (int ct = 0; ct < 7; ++ct) acc[kt][ct] = f32x4{0.f, 0.f, 0.f, 0.f};
      const int vrd = (48 * bb + lc) * 128 + ((lg ^ ((lc >> 1) & 7)) * 16);
      const int sp2 = ((q4 >> 1) & 1) | ((lg & 1) << 1);
      const int wrow = (8 * lg + q4) * 896 + (p4 & 1) * 8;
      v4u va[3], vb[3], wa[4], wb[3];
      auto rdv = [&](v4u (&vf)[3], int c, int ss) {
#pragma unroll
        for (int kt = 0; kt < 3; ++kt) vf[kt] = *(const v4u*)(smem + c * 12288 + ((vrd ^ (64 * ss)) + kt * 2048));
      };
      int wbase[4];
#pragma unroll
      for (int i = 0; i < 4; ++i) wbase[i] = VIMG + wrow + (((14 * cw + 2 * i + (p4 >> 1)) ^ (sp2 << 1)) * 16);
      auto rdw = [&](v4u* wf, int st, int ct0, int n) {
#pragma unroll
        for (int i = 0; i < n; ++i) {
          const unsigned char* p = smem + wbase[i] + ((st & 3) * 28672 + (ct0 ? 128 : 0));
          v2u a = tr_read(p);
          v2u b = tr_read(p + 3584);
          wf[i] = v4u{a[0], a[1], b[0], b[1]};
        }
      };
      auto mm = [&](const v4u (&vf)[3], const v4u* wf, int ct0, int n) {
#pragma unroll
        for (int i = 0; i < n; ++i)
#pragma unroll
          for (int kt = 0; kt < 3; ++kt) acc[kt][ct0 + i] = mfma16(vf[kt], wf[i], acc[kt][ct0 + i]);
      };
#define SB __builtin_amdgcn_sched_barrier(0);
#define IL(n) _Pragma("unroll") for (int i_ = 0; i_ < (n); ++i_) { __builtin_amdgcn_sched_group_barrier(0x8, 1, 0); __builtin_amdgcn_sched_group_barrier(0x100, 1, 0); }
#define IL2(n) _Pragma("unroll") for (int i_ = 0; i_ < (n); ++i_) { __builtin_amdgcn_sched_group_barrier(0x8, 1, 0); __builtin_amdgcn_sched_group_barrier(0x100, 2, 0); }
      unsigned tw = 0;
      RBAR()
      rdv(va, 0, 0);
      rdw(wa, 0, 0, 4);
#pragma unroll
      for (int c = 0; c < 4; ++c) {
        rdw(wb, 2 * c, 4, 3);
        mm(va, wa, 0, 4); IL(6) SB
        RBAR()
        if (c == 1) {
          tw = __builtin_amdgcn_raw_buffer_load_b32(__builtin_amdgcn_make_buffer_rsrc((void*)(ws + (size_t)((2 * bp + bb) * 16 + h) * VW_BLK + cw * 10752), 0, 64, 0x00020000), 0, 0, 0);
          __builtin_amdgcn_sched_barrier(0);
        }
        rdv(vb, c, 1); rdw(wa, 2 * c + 1, 0, 4);
        mm(va, wb, 4, 3); IL2(6) SB
        rdw(wb, 2 * c + 1, 4, 3);
        mm(vb, wa, 0, 4); IL(6) SB
        if (c < 3) {
          RBAR()
          rdv(va, c + 1, 0);
          rdw(wa, 2 * c + 2, 0, 4);
          mm(vb, wb, 4, 3); IL2(6) SB
        } else {
          mm(vb, wb, 4, 3); SB
        }
      }
      asm volatile("" :: "v"(tw));
      const __amdgpu_buffer_rsrc_t rso = __builtin_amdgcn_make_buffer_rsrc((void*)ws, 0, 256 * VW_BLK, 0x00020000);
      {
        const int ob = ((2 * bp + bb) * 16 + h) * VW_BLK;
        const int o1 = ob + 7 * cw * 1024 + lane * 16;
        const int o2 = ob + VW_R2 + 7 * cw * 512 + lane * 8;
#pragma unroll
        for (int ct = 0; ct < 7; ++ct) {
          v4u o;
          o[0] = pack_bf16(acc[0][ct][0], acc[0][ct][1]);
          o[1] = pack_bf16(acc[0][ct][2], acc[0][ct][3]);
          o[2] = pack_bf16(acc[1][ct][0], acc[1][ct][1]);
          o[3] = pack_bf16(acc[1][ct][2], acc[1][ct][3]);
          __builtin_amdgcn_raw_buffer_store_b128(o, rso, o1 + ct * 1024, 0, 16  );
          v2u o3;
          o3[0] = pack_bf16(acc[2][ct][0], acc[2][ct][1]);
          o3[1] = pack_bf16(acc[2][ct][2], acc[2][ct][3]);
          __builtin_amdgcn_raw_buffer_store_b64(o3, rso, o2 + ct * 512, 0, 16  );
        }
      }
    }
  }
}

__global__ __launch_bounds__(512) void gqa_kernel_b(const unsigned char* __restrict__ ws, unsigned char* __restrict__ out) {
  __shared__ __attribute__((aligned(16))) float red[8 * 48 * 36];
  const int bid = blockIdx.x;
  const int xcd = bid & 7, bi = bid >> 3;
  const int b = xcd + 8 * (bi / 14), m = bi % 14;
  const int tid = threadIdx.x, lane = tid & 63;
  const int w = __builtin_amdgcn_readfirstlane(tid >> 6);
  const int lg = lane >> 4, lc = lane & 15;

  v4u p1[2][3], v1[2][2];
  v2u p2[2][3], v2[2][2];
#pragma unroll
  for (int hh = 0; hh < 2; ++hh) {
    const int h = 2 * w + hh;
    const unsigned char* pb = ws + P_OFF + (size_t)(b * 16 + h) * P_BLK;
    const unsigned char* vb = ws + (size_t)(b * 16 + h) * VW_BLK;
#pragma unroll
    for (int ct = 0; ct < 2; ++ct) {
      v1[hh][ct] = *(const v4u*)(vb + (2 * m + ct) * 1024 + lane * 16);
      v2[hh][ct] = *(const v2u*)(vb + VW_R2 + (2 * m + ct) * 512 + lane * 8);
    }
#pragma unroll
    for (int qt = 0; qt < 3; ++qt) {
      p1[hh][qt] = *(const v4u*)(pb + qt * 1024 + lane * 16);
      p2[hh][qt] = *(const v2u*)(pb + P_R2 + qt * 512 + lane * 8);
    }
  }
  __builtin_amdgcn_sched_barrier(0);
#pragma unroll
  for (int qt = 0; qt < 3; ++qt)
#pragma unroll
    for (int ct = 0; ct < 2; ++ct) {
      f32x4 a = f32x4{0.f, 0.f, 0.f, 0.f};
      a = mfma16(p1[0][qt], v1[0][ct], a);
      a = mfma16(p1[1][qt], v1[1][ct], a);
      v4u pa = {p2[0][qt][0], p2[0][qt][1], p2[1][qt][0], p2[1][qt][1]};
      v4u va = {v2[0][ct][0], v2[0][ct][1], v2[1][ct][0], v2[1][ct][1]};
      a = mfma16(pa, va, a);
#pragma unroll
      for (int r = 0; r < 4; ++r) red[(w * 48 + 16 * qt + 4 * lg + r) * 36 + 16 * ct + lc] = a[r];
    }
  __syncthreads();
  if (tid < 384) {
    const int row = tid >> 3, c4 = tid & 7;
    f32x4 s = *(const f32x4*)(red + row * 36 + 4 * c4);
#pragma unroll
    for (int wv = 1; wv < 8; ++wv) s += *(const f32x4*)(red + (wv * 48 + row) * 36 + 4 * c4);
    v2u o = {pack_bf16(s[0], s[1]), pack_bf16(s[2], s[3])};
    *(v2u*)(out + (((size_t)b * SQ + row) * DM + 32 * m + 4 * c4) * 2) = o;
  }
}

extern "C" void kernel_launch(void* const* d_in, const int* in_sizes, int n_in, void* d_out, int out_size, void* d_ws,
                              size_t ws_size, hipStream_t stream) {
  const unsigned char* x = (const unsigned char*)d_in[0];
  const unsigned char* kc = (const unsigned char*)d_in[1];
  const unsigned char* vc = (const unsigned char*)d_in[2];
  const unsigned char* wq = (const unsigned char*)d_in[3];
  const unsigned char* wo = (const unsigned char*)d_in[4];
  unsigned char* ws = (unsigned char*)d_ws;
  gqa_kernel_a<<<256, 768, 0, stream>>>(x, kc, vc, wq, wo, ws, (const unsigned char*)d_out);
  gqa_kernel_b<<<224, 512, 0, stream>>>(ws, (unsigned char*)d_out);
}
```

```cpp
#include <hip/hip_runtime.h>
#include <hip/hip_bf16.h>
#include <stdint.h>

typedef unsigned v4u __attribute__((ext_vector_type(4)));
typedef unsigned v2u __attribute__((ext_vector_type(2)));
typedef short v4s __attribute__((ext_vector_type(4)));
typedef float f32x4 __attribute__((ext_vector_type(4)));
typedef float f32x2 __attribute__((ext_vector_type(2)));
typedef __bf16 bf16x8_t __attribute__((ext_vector_type(8)));
typedef __bf16 bf16x2_t __attribute__((ext_vector_type(2)));
#define LDS_AS __attribute__((address_space(3)))

constexpr int NB = 16, SQ = 48, DM = 448, HQ = 16, HKV = 8, SK = 48, DH = 256;
constexpr int SMEM_A = 163840;
constexpr int QSLOT = 45056;
constexpr int VIMG = 49152;
constexpr int VW_BLK = 43008;
constexpr int VW_R2 = 28672;
constexpr int P_BLK = 4608;
constexpr int P_R2 = 3072;
constexpr size_t P_OFF = (size_t)256 * VW_BLK;

__device__ __forceinline__ unsigned pack_bf16(float a, float b) {
  f32x2 v = {a, b};
  bf16x2_t r = __builtin_convertvector(v, bf16x2_t);
  return __builtin_bit_cast(unsigned, r);
}
__device__ __forceinline__ f32x4 mfma16(v4u a, v4u b, f32x4 c) {
  return __builtin_amdgcn_mfma_f32_16x16x32_bf16(__builtin_bit_cast(bf16x8_t, a), __builtin_bit_cast(bf16x8_t, b), c, 0, 0, 0);
}
__device__ __forceinline__ void swap16(float& a, float& b) { asm volatile("s_nop 1\n\tv_permlane16_swap_b32 %0, %1\n\ts_nop 1" : "+v"(a), "+v"(b)); }
__device__ __forceinline__ void swap32(float& a, float& b) { asm volatile("s_nop 1\n\tv_permlane32_swap_b32 %0, %1\n\ts_nop 1" : "+v"(a), "+v"(b)); }
__device__ __forceinline__ float xlane_max(float v) {
  float a = v, b = v;
  swap16(a, b);
  a = fmaxf(a, b); b = a;
  swap32(a, b);
  return fmaxf(a, b);
}
__device__ __forceinline__ float xlane_sum(float v) {
  float a = v, b = v;
  swap16(a, b);
  a = a + b; b = a;
  swap32(a, b);
  return a + b;
}
__device__ __forceinline__ v2u tr_read(const unsigned char* p) {
  v4s r = __builtin_amdgcn_ds_read_tr16_b64_v4i16((LDS_AS v4s*)p);
  return __builtin_bit_cast(v2u, r);
}
__device__ __forceinline__ void dma16(__amdgpu_buffer_rsrc_t rs, unsigned ldsoff, int voff, int soff) {
  asm volatile("s_nop 4\n\ts_mov_b32 m0, %0\n\ts_nop 0\n\tbuffer_load_dwordx4 %1, %2, %3 offen lds" :: "s"(ldsoff), "v"(voff), "s"(rs), "s"(soff) : "m0", "memory");
}
#define DMA16(RS, LDSOFF, VOFF, SOFF) dma16((RS), lds0 + (LDSOFF), (VOFF), (SOFF))
#define WAITV(n) asm volatile("s_waitcnt vmcnt(" #n ")" ::: "memory")
#define RBAR() { asm volatile("s_waitcnt lgkmcnt(0)" ::: "memory"); __builtin_amdgcn_sched_barrier(0); __builtin_amdgcn_s_barrier(); __builtin_amdgcn_sched_barrier(0); }

constexpr int KIMG = 2 * QSLOT;
__device__ __forceinline__ int next_slot(int cur) { return cur == 0 ? 2 * QSLOT : cur - QSLOT; }

__global__ __launch_bounds__(768, 3) void gqa_kernel_a(const unsigned char* __restrict__ x, const unsigned char* __restrict__ kc,
                                                       const unsigned char* __restrict__ vc, const unsigned char* __restrict__ wq,
                                                       const unsigned char* __restrict__ wo, unsigned char* __restrict__ ws,
                                                       const unsigned char* __restrict__ outp) {
  __shared__ __attribute__((aligned(16))) unsigned char smem[SMEM_A];
  const int bid = blockIdx.x;
  const int xcd = bid & 7, bi = bid >> 3;
  const int type = bi & 1, h = 2 * xcd + ((bi >> 1) & 1), bp = bi >> 2;
  const int gk = h >> 1;
  const int tid = threadIdx.x, lane = tid & 63;
  const int w = __builtin_amdgcn_readfirstlane(tid >> 6);
  const int lg = lane >> 4, lc = lane & 15;
  const int hi = lane >> 5;
  const int q4 = (lane & 15) >> 2, p4 = lane & 3;
  const int bb = (w >> 2) & 1;
  const int cq = w & 3;
  const unsigned lds0 = (unsigned)(size_t)(LDS_AS unsigned char*)smem;

  if (type == 0) {
    if (w >= 8) {
      const int ld = w - 8;
      const __amdgpu_buffer_rsrc_t rsw = __builtin_amdgcn_make_buffer_rsrc((void*)wq, 0, DM * HQ * DH * 2, 0x00020000);
      const __amdgpu_buffer_rsrc_t rsx = __builtin_amdgcn_make_buffer_rsrc((void*)(x + (size_t)bp * (2 * SQ * DM * 2)), 0, 2 * SQ * DM * 2, 0x00020000);
      const __amdgpu_buffer_rsrc_t rsk = __builtin_amdgcn_make_buffer_rsrc((void*)kc, 0, NB * HKV * SK * DH * 2, 0x00020000);
      int wv[4], xv[2];
#pragma unroll
      for (int v = 0; v < 4; ++v) {
        const int sw = (2 * (v & 1) + hi) | ((v >> 1) << 2);
        wv[v] = hi * 8192 + h * 512 + (((lane & 31) ^ (sw << 1)) * 16);
      }
#pragma unroll
      for (int v = 0; v < 2; ++v) xv[v] = (lane >> 3) * 896 + (((lane & 7) ^ ((4 * ((ld + v) & 1) + (lane >> 4)) & 7)) * 16);
      auto issue = [&](int cc, unsigned slot, int lo, int hi_) {
#pragma unroll
        for (int i = 0; i < 3; ++i) if (i >= lo && i < hi_) DMA16(rsx, slot + 32768 + (3 * ld + i) * 1024, xv[i & 1], (3 * ld + i) * 8 * 896 + 128 * cc);
#pragma unroll
        for (int i = 0; i < 8; ++i) if (i + 3 >= lo && i + 3 < hi_)
          DMA16(rsw, slot + ld * 8192 + i * 1024, wv[(i & 1) | (((i >> 2) & 1) << 1)], (2 * cc * 32 + 16 * ld + 2 * i) * 8192);
      };
      issue(0, 0, 0, 11);
      issue(1, 2 * QSLOT, 0, 6);
      WAITV(6); RBAR()
      issue(1, 2 * QSLOT, 6, 11);
      issue(2, QSLOT, 0, 11);
      int islot = 0;
#pragma unroll 1
      for (int c = 1; c < 5; ++c) {
        WAITV(11); RBAR()
        issue(c + 2, islot, 0, 11);
        islot = next_slot(islot);
      }
      WAITV(11);
      RBAR()
      {
#pragma unroll 1
        for (int ii = 0; ii < 12; ++ii) {
          const int i = 12 * (ld & 1) + ii;
          const int kvo = hi * 512 + (((lane & 31) ^ ((2 * i + hi) & 15)) * 16);
          DMA16(rsk, KIMG + (ld >> 1) * 24576 + i * 1024, kvo, ((2 * bp + (ld >> 1)) * HKV + gk) * (SK * DH * 2) + i * 1024);
        }
      }
      WAITV(12);
      RBAR()
      WAITV(0);
      RBAR()
      RBAR()
    } else {
      f32x4 acc[3][4];
#pragma unroll
      for (int qt = 0; qt < 3; ++qt)
#pragma unroll
        for (int j = 0; j < 4; ++j) acc[qt][j] = f32x4{0.f, 0.f, 0.f, 0.f};
      const int xrd = 32768 + (48 * bb + lc) * 128 + ((lg ^ ((lc >> 1) & 7)) * 16);
      const int srd = q4 | ((lg & 1) << 2);
      const int wrd = (8 * lg + q4) * 512 + (((8 * cq + (p4 >> 1)) ^ (srd << 1)) * 16) + (p4 & 1) * 8;
      int cslot = 0;
#define ILQ(n) _Pragma("unroll") for (int i_ = 0; i_ < (n); ++i_) { __builtin_amdgcn_sched_group_barrier(0x8, 1, 0); __builtin_amdgcn_sched_group_barrier(0x100, 1, 0); }
      v4u xa[3], xb2[3], wa[4], wb[4], ka[3][2];
      auto rd = [&](v4u (&xf)[3], v4u (&wf)[4], const unsigned char* slot, int ss) {
#pragma unroll
        for (int qt = 0; qt < 3; ++qt) xf[qt] = *(const v4u*)(slot + ((xrd ^ (64 * ss)) + qt * 2048));
#pragma unroll
        for (int j = 0; j < 4; ++j) {
          v2u a = tr_read(slot + ss * 16384 + (wrd ^ (32 * j)));
          v2u b = tr_read(slot + ss * 16384 + (wrd ^ (32 * j)) + 2048);
          wf[j] = v4u{a[0], a[1], b[0], b[1]};
        }
      };
      auto mm = [&](const v4u (&xf)[3], const v4u (&wf)[4]) {
#pragma unroll
        for (int j = 0; j < 4; ++j)
#pragma unroll
          for (int qt = 0; qt < 3; ++qt) acc[qt][j] = mfma16(wf[j], xf[qt], acc[qt][j]);
      };
      unsigned tw = 0;
      RBAR()
      rd(xa, wa, smem, 0);
#pragma unroll
      for (int c = 0; c < 7; ++c) {
        const unsigned char* slot = smem + cslot;
        rd(xb2, wb, slot, 1);
        mm(xa, wa);
        ILQ(11)
        __builtin_amdgcn_sched_barrier(0);
        cslot = next_slot(cslot);
        RBAR()
        if (c == 1) {
          const unsigned char* ta;
          if (w < 2)      ta = kc + (size_t)((2 * bp + w) * HKV + gk) * (SK * DH * 2);
          else if (w < 4) ta = ws + P_OFF + (size_t)((2 * bp + (w - 2)) * 16 + h) * P_BLK;
          else            ta = outp + (size_t)(xcd + 8 * (w & 1)) * (SQ * DM * 2) + (bi & 15) * 2688;
          tw = __builtin_amdgcn_raw_buffer_load_b32(__builtin_amdgcn_make_buffer_rsrc((void*)ta, 0, 64, 0x00020000), 0, 0, 0);
          __builtin_amdgcn_sched_barrier(0);
        }
        if (c < 6) rd(xa, wa, smem + cslot, 0);
        else {
          const unsigned char* kl = smem + KIMG + (48 * bb + lc) * 512 + (lg & 1) * 8;
#pragma unroll
          for (int kt = 0; kt < 3; ++kt)
#pragma unroll
            for (int f = 0; f < 2; ++f) {
              v2u a = *(const v2u*)(kl + kt * 8192 + (((8 * cq + 4 * f + (lg >> 1)) ^ lc) * 16));
              v2u b = *(const v2u*)(kl + kt * 8192 + (((8 * cq + 4 * f + 2 + (lg >> 1)) ^ lc) * 16));
              ka[kt][f] = v4u{a[0], a[1], b[0], b[1]};
            }
        }
        mm(xb2, wb);
        ILQ(11)
        __builtin_amdgcn_sched_barrier(0);
      }
      f32x4 sacc[3][3];
      {
#pragma unroll
        for (int qt = 0; qt < 3; ++qt) {
#pragma unroll
          for (int kt = 0; kt < 3; ++kt) sacc[kt][qt] = f32x4{0.f, 0.f, 0.f, 0.f};
#pragma unroll
          for (int f = 0; f < 2; ++f) {
            v4u qf;
            qf[0] = pack_bf16(acc[qt][2 * f][0], acc[qt][2 * f][1]);
            qf[1] = pack_bf16(acc[qt][2 * f][2], acc[qt][2 * f][3]);
            qf[2] = pack_bf16(acc[qt][2 * f + 1][0], acc[qt][2 * f + 1][1]);
            qf[3] = pack_bf16(acc[qt][2 * f + 1][2], acc[qt][2 * f + 1][3]);
#pragma unroll
            for (int kt = 0; kt < 3; ++kt) sacc[kt][qt] = mfma16(ka[kt][f], qf, sacc[kt][qt]);
          }
        }
      }
      asm volatile("" :: "v"(tw));
      unsigned char* sp = smem;
#pragma unroll
      for (int kt = 0; kt < 3; ++kt)
#pragma unroll
        for (int qt = 0; qt < 3; ++qt) *(f32x4*)(sp + ((w * 9 + qt * 3 + kt) * 64 + lane) * 16) = sacc[kt][qt];
      RBAR()
      if (w < 6) {
        const int b2 = w >= 3 ? 1 : 0, qt = w - 3 * b2;
        f32x4 pr[4][3];
        const unsigned char* spl = sp + ((36 * b2 + qt * 3) * 64 + lane) * 16;
#pragma unroll
        for (int wv = 0; wv < 4; ++wv)
#pragma unroll
          for (int kt = 0; kt < 3; ++kt) pr[wv][kt] = *(const f32x4*)(spl + (wv * 9 + kt) * 1024);
        __builtin_amdgcn_sched_barrier(0);
        float sv[12];
#pragma unroll
        for (int kt = 0; kt < 3; ++kt) {
          const f32x4 a = (pr[0][kt] + pr[1][kt]) + (pr[2][kt] + pr[3][kt]);
          const unsigned u0 = pack_bf16(a[0], a[1]), u1 = pack_bf16(a[2], a[3]);
          sv[4 * kt + 0] = __builtin_bit_cast(float, u0 << 16);
          sv[4 * kt + 1] = __builtin_bit_cast(float, u0 & 0xffff0000u);
          sv[4 * kt + 2] = __builtin_bit_cast(float, u1 << 16);
          sv[4 * kt + 3] = __builtin_bit_cast(float, u1 & 0xffff0000u);
        }
        float m = fmaxf(fmaxf(fmaxf(sv[0], sv[1]), fmaxf(sv[2], sv[3])), fmaxf(fmaxf(sv[4], sv[5]), fmaxf(sv[6], sv[7])));
        m = fmaxf(m, fmaxf(fmaxf(sv[8], sv[9]), fmaxf(sv[10], sv[11])));
        m = xlane_max(m);
        const float cs = 0.0625f * 1.44269504088896340736f;
        const float mc = -m * cs;
        float sum = 0.f;
#pragma unroll
        for (int i = 0; i < 12; ++i) {
          sv[i] = __builtin_amdgcn_exp2f(__builtin_fmaf(sv[i], cs, mc));
          sum += sv[i];
        }
        sum = xlane_sum(sum);
        const float inv = __builtin_amdgcn_rcpf(sum);
        v4u p1;
        v2u p2;
        p1[0] = pack_bf16(sv[0] * inv, sv[1] * inv);
        p1[1] = pack_bf16(sv[2] * inv, sv[3] * inv);
        p1[2] = pack_bf16(sv[4] * inv, sv[5] * inv);
        p1[3] = pack_bf16(sv[6] * inv, sv[7] * inv);
        p2[0] = pack_bf16(sv[8] * inv, sv[9] * inv);
        p2[1] = pack_bf16(sv[10] * inv, sv[11] * inv);
        unsigned char* pb = ws + P_OFF + (size_t)((2 * bp + b2) * 16 + h) * P_BLK;
        *(v4u*)(pb + qt * 1024 + lane * 16) = p1;
        *(v2u*)(pb + P_R2 + qt * 512 + lane * 8) = p2;
      }
    }
  } else {
    if (w >= 8) {
      const int ld = w - 8;
      const __amdgpu_buffer_rsrc_t rs = __builtin_amdgcn_make_buffer_rsrc((void*)wo, 0, DM * HQ * DH * 2, 0x00020000);
      const __amdgpu_buffer_rsrc_t rsv = __builtin_amdgcn_make_buffer_rsrc((void*)vc, 0, NB * HKV * SK * DH * 2, 0x00020000);
      int vv[2];
#pragma unroll
      for (int v = 0; v < 2; ++v) vv[v] = (lane >> 3) * 512 + (((lane & 7) ^ ((4 * ((ld + v) & 1) + (lane >> 4)) & 7)) * 16);
      int wo_voff[7];
#pragma unroll
      for (int r = 0; r < 7; ++r) {
        const int P = 1024 * r + 16 * lane;
        const int row = P / 896;
        const int phys = (P - 896 * row) >> 4;
        const int sp_ = ((row >> 1) & 1) | ((ld & 1) << 1);
        wo_voff[r] = row * 896 + ((phys ^ (sp_ << 1)) * 16);
      }
      auto issue = [&](int st) {
        if ((st & 1) == 0) {
#pragma unroll
          for (int i = 0; i < 3; ++i)
            DMA16(rsv, (st >> 1) * 12288 + (3 * ld + i) * 1024, vv[i & 1],
                  ((2 * bp + (ld >> 1)) * HKV + gk) * (SK * DH * 2) + (3 * (ld & 1) + i) * 8 * 512 + 64 * st);
        }
        const unsigned slot = VIMG + (st & 3) * 28672 + ld * 7168;
#pragma unroll
        for (int r = 0; r < 7; ++r) DMA16(rs, slot + r * 1024, wo_voff[r], (h * 256 + 32 * st + 8 * ld) * 896);
      };
      issue(0); issue(1);
      WAITV(7);  RBAR() issue(2); issue(3);
      WAITV(17); RBAR() issue(4);
      WAITV(17); RBAR() issue(5);
      WAITV(17); RBAR() issue(6);
      WAITV(17); RBAR() issue(7);
      WAITV(17); RBAR()
      WAITV(7);  RBAR()
      WAITV(0);  RBAR()
    } else {
      const int cw = cq;
      f32x4 acc[3][7];
#pragma unroll
      for (int kt = 0; kt < 3; ++kt)
#pragma unroll
        for (int ct = 0; ct < 7; ++ct) acc[kt][ct] = f32x4{0.f, 0.f, 0.f, 0.f};
      const int vrd = (48 * bb + lc) * 128 + ((lg ^ ((lc >> 1) & 7)) * 16);
      const int sp2 = ((q4 >> 1) & 1) | ((lg & 1) << 1);
      const int wrow = (8 * lg + q4) * 896 + (p4 & 1) * 8;
      v4u va[3], vb[3], wa[4], wb[3];
      auto rdv = [&](v4u (&vf)[3], int c, int ss) {
#pragma unroll
        for (int kt = 0; kt < 3; ++kt) vf[kt] = *(const v4u*)(smem + c * 12288 + ((vrd ^ (64 * ss)) + kt * 2048));
      };
      int wbase[4];
#pragma unroll
      for (int i = 0; i < 4; ++i) wbase[i] = VIMG + wrow + (((14 * cw + 2 * i + (p4 >> 1)) ^ (sp2 << 1)) * 16);
      auto rdw = [&](v4u* wf, int st, int ct0, int n) {
#pragma unroll
        for (int i = 0; i < n; ++i) {
          const unsigned char* p = smem + wbase[i] + ((st & 3) * 28672 + (ct0 ? 128 : 0));
          v2u a = tr_read(p);
          v2u b = tr_read(p + 3584);
          wf[i] = v4u{a[0], a[1], b[0], b[1]};
        }
      };
      auto mm = [&](const v4u (&vf)[3], const v4u* wf, int ct0, int n) {
#pragma unroll
        for (int i = 0; i < n; ++i)
#pragma unroll
          for (int kt = 0; kt < 3; ++kt) acc[kt][ct0 + i] = mfma16(vf[kt], wf[i], acc[kt][ct0 + i]);
      };
#define SB __builtin_amdgcn_sched_barrier(0);
#define IL(n) _Pragma("unroll") for (int i_ = 0; i_ < (n); ++i_) { __builtin_amdgcn_sched_group_barrier(0x8, 1, 0); __builtin_amdgcn_sched_group_barrier(0x100, 1, 0); }
#define IL2(n) _Pragma("unroll") for (int i_ = 0; i_ < (n); ++i_) { __builtin_amdgcn_sched_group_barrier(0x8, 1, 0); __builtin_amdgcn_sched_group_barrier(0x100, 2, 0); }
      unsigned tw = 0;
      RBAR()
      rdv(va, 0, 0);
      rdw(wa, 0, 0, 4);
#pragma unroll
      for (int c = 0; c < 4; ++c) {
        rdw(wb, 2 * c, 4, 3);
        mm(va, wa, 0, 4); IL(6) SB
        RBAR()
        if (c == 1) {
          tw = __builtin_amdgcn_raw_buffer_load_b32(__builtin_amdgcn_make_buffer_rsrc((void*)(ws + (size_t)((2 * bp + bb) * 16 + h) * VW_BLK + cw * 10752), 0, 64, 0x00020000), 0, 0, 0);
          __builtin_amdgcn_sched_barrier(0);
        }
        rdv(vb, c, 1); rdw(wa, 2 * c + 1, 0, 4);
        mm(va, wb, 4, 3); IL2(6) SB
        rdw(wb, 2 * c + 1, 4, 3);
        mm(vb, wa, 0, 4); IL(6) SB
        if (c < 3) {
          RBAR()
          rdv(va, c + 1, 0);
          rdw(wa, 2 * c + 2, 0, 4);
          mm(vb, wb, 4, 3); IL2(6) SB
        } else {
          mm(vb, wb, 4, 3); SB
        }
      }
      asm volatile("" :: "v"(tw));
      __builtin_amdgcn_s_sleep(64);
      const __amdgpu_buffer_rsrc_t rso = __builtin_amdgcn_make_buffer_rsrc((void*)ws, 0, 256 * VW_BLK, 0x00020000);
      {
        const int ob = ((2 * bp + bb) * 16 + h) * VW_BLK;
        const int o1 = ob + 7 * cw * 1024 + lane * 16;
        const int o2 = ob + VW_R2 + 7 * cw * 512 + lane * 8;
#pragma unroll
        for (int ct = 0; ct < 7; ++ct) {
          v4u o;
          o[0] = pack_bf16(acc[0][ct][0], acc[0][ct][1]);
          o[1] = pack_bf16(acc[0][ct][2], acc[0][ct][3]);
          o[2] = pack_bf16(acc[1][ct][0], acc[1][ct][1]);
          o[3] = pack_bf16(acc[1][ct][2], acc[1][ct][3]);
          __builtin_amdgcn_raw_buffer_store_b128(o, rso, o1 + ct * 1024, 0, 16  );
          v2u o3;
          o3[0] = pack_bf16(acc[2][ct][0], acc[2][ct][1]);
          o3[1] = pack_bf16(acc[2][ct][2], acc[2][ct][3]);
          __builtin_amdgcn_raw_buffer_store_b64(o3, rso, o2 + ct * 512, 0, 16  );
        }
      }
    }
  }
}

__global__ __launch_bounds__(512) void gqa_kernel_b(const unsigned char* __restrict__ ws, unsigned char* __restrict__ out) {
  __shared__ __attribute__((aligned(16))) float red[8 * 48 * 36];
  const int bid = blockIdx.x;
  const int xcd = bid & 7, bi = bid >> 3;
  const int b = xcd + 8 * (bi / 14), m = bi % 14;
  const int tid = threadIdx.x, lane = tid & 63;
  const int w = __builtin_amdgcn_readfirstlane(tid >> 6);
  const int lg = lane >> 4, lc = lane & 15;

  v4u p1[2][3], v1[2][2];
  v2u p2[2][3], v2[2][2];
#pragma unroll
  for (int hh = 0; hh < 2; ++hh) {
    const int h = 2 * w + hh;
    const unsigned char* pb = ws + P_OFF + (size_t)(b * 16 + h) * P_BLK;
    const unsigned char* vb = ws + (size_t)(b * 16 + h) * VW_BLK;
#pragma unroll
    for (int ct = 0; ct < 2; ++ct) {
      v1[hh][ct] = *(const v4u*)(vb + (2 * m + ct) * 1024 + lane * 16);
      v2[hh][ct] = *(const v2u*)(vb + VW_R2 + (2 * m + ct) * 512 + lane * 8);
    }
#pragma unroll
    for (int qt = 0; qt < 3; ++qt) {
      p1[hh][qt] = *(const v4u*)(pb + qt * 1024 + lane * 16);
      p2[hh][qt] = *(const v2u*)(pb + P_R2 + qt * 512 + lane * 8);
    }
  }
  __builtin_amdgcn_sched_barrier(0);
#pragma unroll
  for (int qt = 0; qt < 3; ++qt)
#pragma unroll
    for (int ct = 0; ct < 2; ++ct) {
      f32x4 a = f32x4{0.f, 0.f, 0.f, 0.f};
      a = mfma16(p1[0][qt], v1[0][ct], a);
      a = mfma16(p1[1][qt], v1[1][ct], a);
      v4u pa = {p2[0][qt][0], p2[0][qt][1], p2[1][qt][0], p2[1][qt][1]};
      v4u va = {v2[0][ct][0], v2[0][ct][1], v2[1][ct][0], v2[1][ct][1]};
      a = mfma16(pa, va, a);
#pragma unroll
      for (int r = 0; r < 4; ++r) red[(w * 48 + 16 * qt + 4 * lg + r) * 36 + 16 * ct + lc] = a[r];
    }
  __syncthreads();
  if (tid < 384) {
    const int row = tid >> 3, c4 = tid & 7;
    f32x4 s = *(const f32x4*)(red + row * 36 + 4 * c4);
#pragma unroll
    for (int wv = 1; wv < 8; ++wv) s += *(const f32x4*)(red + (wv * 48 + row) * 36 + 4 * c4);
    v2u o = {pack_bf16(s[0], s[1]), pack_bf16(s[2], s[3])};
    *(v2u*)(out + (((size_t)b * SQ + row) * DM + 32 * m + 4 * c4) * 2) = o;
  }
}

extern "C" void kernel_launch(void* const* d_in, const int* in_sizes, int n_in, void* d_out, int out_size, void* d_ws,
                              size_t ws_size, hipStream_t stream) {
  const unsigned char* x = (const unsigned char*)d_in[0];
  const unsigned char* kc = (const unsigned char*)d_in[1];
  const unsigned char* vc = (const unsigned char*)d_in[2];
  const unsigned char* wq = (const unsigned char*)d_in[3];
  const unsigned char* wo = (const unsigned char*)d_in[4];
  unsigned char* ws = (unsigned char*)d_ws;
  gqa_kernel_a<<<256, 768, 0, stream>>>(x, kc, vc, wq, wo, ws, (const unsigned char*)d_out);
  gqa_kernel_b<<<224, 512, 0, stream>>>(ws, (unsigned char*)d_out);
}
```

```cpp
#include <hip/hip_runtime.h>
#include <hip/hip_bf16.h>
#include <stdint.h>

typedef unsigned v4u __attribute__((ext_vector_type(4)));
typedef unsigned v2u __attribute__((ext_vector_type(2)));
typedef short v4s __attribute__((ext_vector_type(4)));
typedef float f32x4 __attribute__((ext_vector_type(4)));
typedef float f32x2 __attribute__((ext_vector_type(2)));
typedef __bf16 bf16x8_t __attribute__((ext_vector_type(8)));
typedef __bf16 bf16x2_t __attribute__((ext_vector_type(2)));
#define LDS_AS __attribute__((address_space(3)))

constexpr int NB = 16, SQ = 48, DM = 448, HQ = 16, HKV = 8, SK = 48, DH = 256;
constexpr int SMEM_A = 163840;
constexpr int QSLOT = 45056;
constexpr int VIMG = 49152;
constexpr int VW_BLK = 43008;
constexpr int VW_R2 = 28672;
constexpr int P_BLK = 4608;
constexpr int P_R2 = 3072;
constexpr size_t P_OFF = (size_t)256 * VW_BLK;

__device__ __forceinline__ unsigned pack_bf16(float a, float b) {
  f32x2 v = {a, b};
  bf16x2_t r = __builtin_convertvector(v, bf16x2_t);
  return __builtin_bit_cast(unsigned, r);
}
__device__ __forceinline__ f32x4 mfma16(v4u a, v4u b, f32x4 c) {
  return __builtin_amdgcn_mfma_f32_16x16x32_bf16(__builtin_bit_cast(bf16x8_t, a), __builtin_bit_cast(bf16x8_t, b), c, 0, 0, 0);
}
__device__ __forceinline__ void swap16(float& a, float& b) { asm volatile("s_nop 1\n\tv_permlane16_swap_b32 %0, %1\n\ts_nop 1" : "+v"(a), "+v"(b)); }
__device__ __forceinline__ void swap32(float& a, float& b) { asm volatile("s_nop 1\n\tv_permlane32_swap_b32 %0, %1\n\ts_nop 1" : "+v"(a), "+v"(b)); }
__device__ __forceinline__ float xlane_max(float v) {
  float a = v, b = v;
  swap16(a, b);
  a = fmaxf(a, b); b = a;
  swap32(a, b);
  return fmaxf(a, b);
}
__device__ __forceinline__ float xlane_sum(float v) {
  float a = v, b = v;
  swap16(a, b);
  a = a + b; b = a;
  swap32(a, b);
  return a + b;
}
__device__ __forceinline__ v2u tr_read(const unsigned char* p) {
  v4s r = __builtin_amdgcn_ds_read_tr16_b64_v4i16((LDS_AS v4s*)p);
  return __builtin_bit_cast(v2u, r);
}
__device__ __forceinline__ void dma16(__amdgpu_buffer_rsrc_t rs, unsigned ldsoff, int voff, int soff) {
  asm volatile("s_nop 4\n\ts_mov_b32 m0, %0\n\ts_nop 0\n\tbuffer_load_dwordx4 %1, %2, %3 offen lds" :: "s"(ldsoff), "v"(voff), "s"(rs), "s"(soff) : "m0", "memory");
}
#define DMA16(RS, LDSOFF, VOFF, SOFF) dma16((RS), lds0 + (LDSOFF), (VOFF), (SOFF))
#define WAITV(n) asm volatile("s_waitcnt vmcnt(" #n ")" ::: "memory")
#define RBAR() { asm volatile("s_waitcnt lgkmcnt(0)" ::: "memory"); __builtin_amdgcn_sched_barrier(0); __builtin_amdgcn_s_barrier(); __builtin_amdgcn_sched_barrier(0); }

constexpr int KIMG = 2 * QSLOT;
__device__ __forceinline__ int next_slot(int cur) { return cur == 0 ? 2 * QSLOT : cur - QSLOT; }

__global__ __launch_bounds__(768, 3) void gqa_kernel_a(const unsigned char* __restrict__ x, const unsigned char* __restrict__ kc,
                                                       const unsigned char* __restrict__ vc, const unsigned char* __restrict__ wq,
                                                       const unsigned char* __restrict__ wo, unsigned char* __restrict__ ws,
                                                       const unsigned char* __restrict__ outp) {
  __shared__ __attribute__((aligned(16))) unsigned char smem[SMEM_A];
  const int bid = blockIdx.x;
  const int xcd = bid & 7, bi = bid >> 3;
  const int type = bi & 1, h = 2 * xcd + ((bi >> 1) & 1), bp = bi >> 2;
  const int gk = h >> 1;
  const int tid = threadIdx.x, lane = tid & 63;
  const int w = __builtin_amdgcn_readfirstlane(tid >> 6);
  const int lg = lane >> 4, lc = lane & 15;
  const int hi = lane >> 5;
  const int q4 = (lane & 15) >> 2, p4 = lane & 3;
  const int bb = (w >> 2) & 1;
  const int cq = w & 3;
  const unsigned lds0 = (unsigned)(size_t)(LDS_AS unsigned char*)smem;

  if (type == 0) {
    if (w >= 8) {
      const int ld = w - 8;
      const __amdgpu_buffer_rsrc_t rsw = __builtin_amdgcn_make_buffer_rsrc((void*)wq, 0, DM * HQ * DH * 2, 0x00020000);
      const __amdgpu_buffer_rsrc_t rsx = __builtin_amdgcn_make_buffer_rsrc((void*)(x + (size_t)bp * (2 * SQ * DM * 2)), 0, 2 * SQ * DM * 2, 0x00020000);
      const __amdgpu_buffer_rsrc_t rsk = __builtin_amdgcn_make_buffer_rsrc((void*)kc, 0, NB * HKV * SK * DH * 2, 0x00020000);
      int wv[4], xv[2];
#pragma unroll
      for (int v = 0; v < 4; ++v) {
        const int sw = (2 * (v & 1) + hi) | ((v >> 1) << 2);
        wv[v] = hi * 8192 + h * 512 + (((lane & 31) ^ (sw << 1)) * 16);
      }
#pragma unroll
      for (int v = 0; v < 2; ++v) xv[v] = (lane >> 3) * 896 + (((lane & 7) ^ ((4 * ((ld + v) & 1) + (lane >> 4)) & 7)) * 16);
      auto issue = [&](int cc, unsigned slot, int lo, int hi_) {
#pragma unroll
        for (int i = 0; i < 3; ++i) if (i >= lo && i < hi_) DMA16(rsx, slot + 32768 + (3 * ld + i) * 1024, xv[i & 1], (3 * ld + i) * 8 * 896 + 128 * cc);
#pragma unroll
        for (int i = 0; i < 8; ++i) if (i + 3 >= lo && i + 3 < hi_)
          DMA16(rsw, slot + ld * 8192 + i * 1024, wv[(i & 1) | (((i >> 2) & 1) << 1)], (2 * cc * 32 + 16 * ld + 2 * i) * 8192);
      };
      issue(0, 0, 0, 11);
      issue(1, 2 * QSLOT, 0, 6);
      WAITV(6); RBAR()
      issue(1, 2 * QSLOT, 6, 11);
      issue(2, QSLOT, 0, 11);
      WAITV(11); RBAR()
      issue(3, 0, 0, 11);
      v4u kreg[12];
#pragma unroll
      for (int ii = 0; ii < 12; ++ii)
        kreg[ii] = __builtin_amdgcn_raw_buffer_load_b128(rsk, lane * 16, ((2 * bp + (ld >> 1)) * HKV + gk) * (SK * DH * 2) + (12 * (ld & 1) + ii) * 1024, 0);
      __builtin_amdgcn_sched_barrier(0);
      WAITV(23); RBAR()
      issue(4, 2 * QSLOT, 0, 11);
      WAITV(23); RBAR()
      issue(5, QSLOT, 0, 11);
      WAITV(11); RBAR()
      issue(6, 0, 0, 11);
      WAITV(11); RBAR()
#pragma unroll
      for (int ii = 0; ii < 12; ++ii) {
        const int row = 2 * (12 * (ld & 1) + ii) + hi;
        *(v4u*)(smem + KIMG + (ld >> 1) * 24576 + row * 512 + (((lane & 31) ^ (row & 15)) * 16)) = kreg[ii];
      }
      WAITV(0);
      RBAR()
      RBAR()
      RBAR()
    } else {
      f32x4 acc[3][4];
#pragma unroll
      for (int qt = 0; qt < 3; ++qt)
#pragma unroll
        for (int j = 0; j < 4; ++j) acc[qt][j] = f32x4{0.f, 0.f, 0.f, 0.f};
      const int xrd = 32768 + (48 * bb + lc) * 128 + ((lg ^ ((lc >> 1) & 7)) * 16);
      const int srd = q4 | ((lg & 1) << 2);
      const int wrd = (8 * lg + q4) * 512 + (((8 * cq + (p4 >> 1)) ^ (srd << 1)) * 16) + (p4 & 1) * 8;
      int cslot = 0;
#define ILQ(n) _Pragma("unroll") for (int i_ = 0; i_ < (n); ++i_) { __builtin_amdgcn_sched_group_barrier(0x8, 1, 0); __builtin_amdgcn_sched_group_barrier(0x100, 1, 0); }
      v4u xa[3], xb2[3], wa[4], wb[4], ka[3][2];
      auto rd = [&](v4u (&xf)[3], v4u (&wf)[4], const unsigned char* slot, int ss) {
#pragma unroll
        for (int qt = 0; qt < 3; ++qt) xf[qt] = *(const v4u*)(slot + ((xrd ^ (64 * ss)) + qt * 2048));
#pragma unroll
        for (int j = 0; j < 4; ++j) {
          v2u a = tr_read(slot + ss * 16384 + (wrd ^ (32 * j)));
          v2u b = tr_read(slot + ss * 16384 + (wrd ^ (32 * j)) + 2048);
          wf[j] = v4u{a[0], a[1], b[0], b[1]};
        }
      };
      auto mm = [&](const v4u (&xf)[3], const v4u (&wf)[4]) {
#pragma unroll
        for (int j = 0; j < 4; ++j)
#pragma unroll
          for (int qt = 0; qt < 3; ++qt) acc[qt][j] = mfma16(wf[j], xf[qt], acc[qt][j]);
      };
      unsigned tw = 0;
      RBAR()
      rd(xa, wa, smem, 0);
#pragma unroll
      for (int c = 0; c < 7; ++c) {
        const unsigned char* slot = smem + cslot;
        rd(xb2, wb, slot, 1);
        mm(xa, wa);
        ILQ(11)
        __builtin_amdgcn_sched_barrier(0);
        cslot = next_slot(cslot);
        RBAR()
        if (c == 1) {
          const unsigned char* ta;
          if (w < 2)      ta = kc + (size_t)((2 * bp + w) * HKV + gk) * (SK * DH * 2);
          else if (w < 4) ta = ws + P_OFF + (size_t)((2 * bp + (w - 2)) * 16 + h) * P_BLK;
          else            ta = outp + (size_t)(xcd + 8 * (w & 1)) * (SQ * DM * 2) + (bi & 15) * 2688;
          tw = __builtin_amdgcn_raw_buffer_load_b32(__builtin_amdgcn_make_buffer_rsrc((void*)ta, 0, 64, 0x00020000), 0, 0, 0);
          __builtin_amdgcn_sched_barrier(0);
        }
        if (c < 6) rd(xa, wa, smem + cslot, 0);
        else {
          const unsigned char* kl = smem + KIMG + (48 * bb + lc) * 512 + (lg & 1) * 8;
#pragma unroll
          for (int kt = 0; kt < 3; ++kt)
#pragma unroll
            for (int f = 0; f < 2; ++f) {
              v2u a = *(const v2u*)(kl + kt * 8192 + (((8 * cq + 4 * f + (lg >> 1)) ^ lc) * 16));
              v2u b = *(const v2u*)(kl + kt * 8192 + (((8 * cq + 4 * f + 2 + (lg >> 1)) ^ lc) * 16));
              ka[kt][f] = v4u{a[0], a[1], b[0], b[1]};
            }
        }
        mm(xb2, wb);
        ILQ(11)
        __builtin_amdgcn_sched_barrier(0);
      }
      f32x4 sacc[3][3];
      {
#pragma unroll
        for (int qt = 0; qt < 3; ++qt) {
#pragma unroll
          for (int kt = 0; kt < 3; ++kt) sacc[kt][qt] = f32x4{0.f, 0.f, 0.f, 0.f};
#pragma unroll
          for (int f = 0; f < 2; ++f) {
            v4u qf;
            qf[0] = pack_bf16(acc[qt][2 * f][0], acc[qt][2 * f][1]);
            qf[1] = pack_bf16(acc[qt][2 * f][2], acc[qt][2 * f][3]);
            qf[2] = pack_bf16(acc[qt][2 * f + 1][0], acc[qt][2 * f + 1][1]);
            qf[3] = pack_bf16(acc[qt][2 * f + 1][2], acc[qt][2 * f + 1][3]);
#pragma unroll
            for (int kt = 0; kt < 3; ++kt) sacc[kt][qt] = mfma16(ka[kt][f], qf, sacc[kt][qt]);
          }
        }
      }
      asm volatile("" :: "v"(tw));
      unsigned char* sp = smem;
#pragma unroll
      for (int kt = 0; kt < 3; ++kt)
#pragma unroll
        for (int qt = 0; qt < 3; ++qt) *(f32x4*)(sp + ((w * 9 + qt * 3 + kt) * 64 + lane) * 16) = sacc[kt][qt];
      RBAR()
      if (w < 6) {
        const int b2 = w >= 3 ? 1 : 0, qt = w - 3 * b2;
        f32x4 pr[4][3];
        const unsigned char* spl = sp + ((36 * b2 + qt * 3) * 64 + lane) * 16;
#pragma unroll
        for (int wv = 0; wv < 4; ++wv)
#pragma unroll
          for (int kt = 0; kt < 3; ++kt) pr[wv][kt] = *(const f32x4*)(spl + (wv * 9 + kt) * 1024);
        __builtin_amdgcn_sched_barrier(0);
        float sv[12];
#pragma unroll
        for (int kt = 0; kt < 3; ++kt) {
          const f32x4 a = (pr[0][kt] + pr[1][kt]) + (pr[2][kt] + pr[3][kt]);
          const unsigned u0 = pack_bf16(a[0], a[1]), u1 = pack_bf16(a[2], a[3]);
          sv[4 * kt + 0] = __builtin_bit_cast(float, u0 << 16);
          sv[4 * kt + 1] = __builtin_bit_cast(float, u0 & 0xffff0000u);
          sv[4 * kt + 2] = __builtin_bit_cast(float, u1 << 16);
          sv[4 * kt + 3] = __builtin_bit_cast(float, u1 & 0xffff0000u);
        }
        float m = fmaxf(fmaxf(fmaxf(sv[0], sv[1]), fmaxf(sv[2], sv[3])), fmaxf(fmaxf(sv[4], sv[5]), fmaxf(sv[6], sv[7])));
        m = fmaxf(m, fmaxf(fmaxf(sv[8], sv[9]), fmaxf(sv[10], sv[11])));
        m = xlane_max(m);
        const float cs = 0.0625f * 1.44269504088896340736f;
        const float mc = -m * cs;
        float sum = 0.f;
#pragma unroll
        for (int i = 0; i < 12; ++i) {
          sv[i] = __builtin_amdgcn_exp2f(__builtin_fmaf(sv[i], cs, mc));
          sum += sv[i];
        }
        sum = xlane_sum(sum);
        const float inv = __builtin_amdgcn_rcpf(sum);
        v4u p1;
        v2u p2;
        p1[0] = pack_bf16(sv[0] * inv, sv[1] * inv);
        p1[1] = pack_bf16(sv[2] * inv, sv[3] * inv);
        p1[2] = pack_bf16(sv[4] * inv, sv[5] * inv);
        p1[3] = pack_bf16(sv[6] * inv, sv[7] * inv);
        p2[0] = pack_bf16(sv[8] * inv, sv[9] * inv);
        p2[1] = pack_bf16(sv[10] * inv, sv[11] * inv);
        unsigned char* pb = ws + P_OFF + (size_t)((2 * bp + b2) * 16 + h) * P_BLK;
        *(v4u*)(pb + qt * 1024 + lane * 16) = p1;
        *(v2u*)(pb + P_R2 + qt * 512 + lane * 8) = p2;
      }
    }
  } else {
    if (w >= 8) {
      const int ld = w - 8;
      const __amdgpu_buffer_rsrc_t rs = __builtin_amdgcn_make_buffer_rsrc((void*)wo, 0, DM * HQ * DH * 2, 0x00020000);
      const __amdgpu_buffer_rsrc_t rsv = __builtin_amdgcn_make_buffer_rsrc((void*)vc, 0, NB * HKV * SK * DH * 2, 0x00020000);
      int vv[2];
#pragma unroll
      for (int v = 0; v < 2; ++v) vv[v] = (lane >> 3) * 512 + (((lane & 7) ^ ((4 * ((ld + v) & 1) + (lane >> 4)) & 7)) * 16);
      int wo_voff[7];
#pragma unroll
      for (int r = 0; r < 7; ++r) {
        const int P = 1024 * r + 16 * lane;
        const int row = P / 896;
        const int phys = (P - 896 * row) >> 4;
        const int sp_ = ((row >> 1) & 1) | ((ld & 1) << 1);
        wo_voff[r] = row * 896 + ((phys ^ (sp_ << 1)) * 16);
      }
      auto issue = [&](int st) {
        if ((st & 1) == 0) {
#pragma unroll
          for (int i = 0; i < 3; ++i)
            DMA16(rsv, (st >> 1) * 12288 + (3 * ld + i) * 1024, vv[i & 1],
                  ((2 * bp + (ld >> 1)) * HKV + gk) * (SK * DH * 2) + (3 * (ld & 1) + i) * 8 * 512 + 64 * st);
        }
        const unsigned slot = VIMG + (st & 3) * 28672 + ld * 7168;
#pragma unroll
        for (int r = 0; r < 7; ++r) DMA16(rs, slot + r * 1024, wo_voff[r], (h * 256 + 32 * st + 8 * ld) * 896);
      };
      __builtin_amdgcn_s_sleep(30);
      issue(0); issue(1);
      WAITV(7);  RBAR() issue(2); issue(3);
      WAITV(17); RBAR() issue(4);
      WAITV(17); RBAR() issue(5);
      WAITV(17); RBAR() issue(6);
      WAITV(17); RBAR() issue(7);
      WAITV(17); RBAR()
      WAITV(7);  RBAR()
      WAITV(0);  RBAR()
    } else {
      const int cw = cq;
      f32x4 acc[3][7];
#pragma unroll
      for (int kt = 0; kt < 3; ++kt)
#pragma unroll
        for (int ct = 0; ct < 7; ++ct) acc[kt][ct] = f32x4{0.f, 0.f, 0.f, 0.f};
      const int vrd = (48 * bb + lc) * 128 + ((lg ^ ((lc >> 1) & 7)) * 16);
      const int sp2 = ((q4 >> 1) & 1) | ((lg & 1) << 1);
      const int wrow = (8 * lg + q4) * 896 + (p4 & 1) * 8;
      v4u va[3], vb[3], wa[4], wb[3];
      auto rdv = [&](v4u (&vf)[3], int c, int ss) {
#pragma unroll
        for (int kt = 0; kt < 3; ++kt) vf[kt] = *(const v4u*)(smem + c * 12288 + ((vrd ^ (64 * ss)) + kt * 2048));
      };
      int wbase[4];
#pragma unroll
      for (int i = 0; i < 4; ++i) wbase[i] = VIMG + wrow + (((14 * cw + 2 * i + (p4 >> 1)) ^ (sp2 << 1)) * 16);
      auto rdw = [&](v4u* wf, int st, int ct0, int n) {
#pragma unroll
        for (int i = 0; i < n; ++i) {
          const unsigned char* p = smem + wbase[i] + ((st & 3) * 28672 + (ct0 ? 128 : 0));
          v2u a = tr_read(p);
          v2u b = tr_read(p + 3584);
          wf[i] = v4u{a[0], a[1], b[0], b[1]};
        }
      };
      auto mm = [&](const v4u (&vf)[3], const v4u* wf, int ct0, int n) {
#pragma unroll
        for (int i = 0; i < n; ++i)
#pragma unroll
          for (int kt = 0; kt < 3; ++kt) acc[kt][ct0 + i] = mfma16(vf[kt], wf[i], acc[kt][ct0 + i]);
      };
#define SB __builtin_amdgcn_sched_barrier(0);
#define IL(n) _Pragma("unroll") for (int i_ = 0; i_ < (n); ++i_) { __builtin_amdgcn_sched_group_barrier(0x8, 1, 0); __builtin_amdgcn_sched_group_barrier(0x100, 1, 0); }
#define IL2(n) _Pragma("unroll") for (int i_ = 0; i_ < (n); ++i_) { __builtin_amdgcn_sched_group_barrier(0x8, 1, 0); __builtin_amdgcn_sched_group_barrier(0x100, 2, 0); }
      unsigned tw = 0;
      RBAR()
      rdv(va, 0, 0);
      rdw(wa, 0, 0, 4);
#pragma unroll
      for (int c = 0; c < 4; ++c) {
        rdw(wb, 2 * c, 4, 3);
        mm(va, wa, 0, 4); IL(6) SB
        RBAR()
        if (c == 1) {
          tw = __builtin_amdgcn_raw_buffer_load_b32(__builtin_amdgcn_make_buffer_rsrc((void*)(ws + (size_t)((2 * bp + bb) * 16 + h) * VW_BLK + cw * 10752), 0, 64, 0x00020000), 0, 0, 0);
          __builtin_amdgcn_sched_barrier(0);
        }
        rdv(vb, c, 1); rdw(wa, 2 * c + 1, 0, 4);
        mm(va, wb, 4, 3); IL2(6) SB
        rdw(wb, 2 * c + 1, 4, 3);
        mm(vb, wa, 0, 4); IL(6) SB
        if (c < 3) {
          RBAR()
          rdv(va, c + 1, 0);
          rdw(wa, 2 * c + 2, 0, 4);
          mm(vb, wb, 4, 3); IL2(6) SB
        } else {
          mm(vb, wb, 4, 3); SB
        }
      }
      asm volatile("" :: "v"(tw));
      __builtin_amdgcn_s_sleep(30);
      const __amdgpu_buffer_rsrc_t rso = __builtin_amdgcn_make_buffer_rsrc((void*)ws, 0, 256 * VW_BLK, 0x00020000);
      {
        const int ob = ((2 * bp + bb) * 16 + h) * VW_BLK;
        const int o1 = ob + 7 * cw * 1024 + lane * 16;
        const int o2 = ob + VW_R2 + 7 * cw * 512 + lane * 8;
#pragma unroll
        for (int ct = 0; ct < 7; ++ct) {
          v4u o;
          o[0] = pack_bf16(acc[0][ct][0], acc[0][ct][1]);
          o[1] = pack_bf16(acc[0][ct][2], acc[0][ct][3]);
          o[2] = pack_bf16(acc[1][ct][0], acc[1][ct][1]);
          o[3] = pack_bf16(acc[1][ct][2], acc[1][ct][3]);
          __builtin_amdgcn_raw_buffer_store_b128(o, rso, o1 + ct * 1024, 0, 16  );
          v2u o3;
          o3[0] = pack_bf16(acc[2][ct][0], acc[2][ct][1]);
          o3[1] = pack_bf16(acc[2][ct][2], acc[2][ct][3]);
          __builtin_amdgcn_raw_buffer_store_b64(o3, rso, o2 + ct * 512, 0, 16  );
        }
      }
    }
  }
}

__global__ __launch_bounds__(512) void gqa_kernel_b(const unsigned char* __restrict__ ws, unsigned char* __restrict__ out) {
  __shared__ __attribute__((aligned(16))) float red[8 * 48 * 36];
  const int bid = blockIdx.x;
  const int xcd = bid & 7, bi = bid >> 3;
  const int b = xcd + 8 * (bi / 14), m = bi % 14;
  const int tid = threadIdx.x, lane = tid & 63;
  const int w = __builtin_amdgcn_readfirstlane(tid >> 6);
  const int lg = lane >> 4, lc = lane & 15;

  v4u p1[2][3], v1[2][2];
  v2u p2[2][3], v2[2][2];
#pragma unroll
  for (int hh = 0; hh < 2; ++hh) {
    const int h = 2 * w + hh;
    const unsigned char* pb = ws + P_OFF + (size_t)(b * 16 + h) * P_BLK;
    const unsigned char* vb = ws + (size_t)(b * 16 + h) * VW_BLK;
#pragma unroll
    for (int ct = 0; ct < 2; ++ct) {
      v1[hh][ct] = *(const v4u*)(vb + (2 * m + ct) * 1024 + lane * 16);
      v2[hh][ct] = *(const v2u*)(vb + VW_R2 + (2 * m + ct) * 512 + lane * 8);
    }
#pragma unroll
    for (int qt = 0; qt < 3; ++qt) {
      p1[hh][qt] = *(const v4u*)(pb + qt * 1024 + lane * 16);
      p2[hh][qt] = *(const v2u*)(pb + P_R2 + qt * 512 + lane * 8);
    }
  }
  __builtin_amdgcn_sched_barrier(0);
#pragma unroll
  for (int qt = 0; qt < 3; ++qt)
#pragma unroll
    for (int ct = 0; ct < 2; ++ct) {
      f32x4 a = f32x4{0.f, 0.f, 0.f, 0.f};
      a = mfma16(p1[0][qt], v1[0][ct], a);
      a = mfma16(p1[1][qt], v1[1][ct], a);
      v4u pa = {p2[0][qt][0], p2[0][qt][1], p2[1][qt][0], p2[1][qt][1]};
      v4u va = {v2[0][ct][0], v2[0][ct][1], v2[1][ct][0], v2[1][ct][1]};
      a = mfma16(pa, va, a);
#pragma unroll
      for (int r = 0; r < 4; ++r) red[(w * 48 + 16 * qt + 4 * lg + r) * 36 + 16 * ct + lc] = a[r];
    }
  __syncthreads();
  if (tid < 384) {
    const int row = tid >> 3, c4 = tid & 7;
    f32x4 s = *(const f32x4*)(red + row * 36 + 4 * c4);
#pragma unroll
    for (int wv = 1; wv < 8; ++wv) s += *(const f32x4*)(red + (wv * 48 + row) * 36 + 4 * c4);
    v2u o = {pack_bf16(s[0], s[1]), pack_bf16(s[2], s[3])};
    *(v2u*)(out + (((size_t)b * SQ + row) * DM + 32 * m + 4 * c4) * 2) = o;
  }
}

extern "C" void kernel_launch(void* const* d_in, const int* in_sizes, int n_in, void* d_out, int out_size, void* d_ws,
                              size_t ws_size, hipStream_t stream) {
  const unsigned char* x = (const unsigned char*)d_in[0];
  const unsigned char* kc = (const unsigned char*)d_in[1];
  const unsigned char* vc = (const unsigned char*)d_in[2];
  const unsigned char* wq = (const unsigned char*)d_in[3];
  const unsigned char* wo = (const unsigned char*)d_in[4];
  unsigned char* ws = (unsigned char*)d_ws;
  gqa_kernel_a<<<256, 768, 0, stream>>>(x, kc, vc, wq, wo, ws, (const unsigned char*)d_out);
  gqa_kernel_b<<<224, 512, 0, stream>>>(ws, (unsigned char*)d_out);
}
```

```cpp
#include <hip/hip_runtime.h>
#include <hip/hip_bf16.h>
#include <stdint.h>

typedef unsigned v4u __attribute__((ext_vector_type(4)));
typedef unsigned v2u __attribute__((ext_vector_type(2)));
typedef short v4s __attribute__((ext_vector_type(4)));
typedef float f32x4 __attribute__((ext_vector_type(4)));
typedef float f32x2 __attribute__((ext_vector_type(2)));
typedef __bf16 bf16x8_t __attribute__((ext_vector_type(8)));
typedef __bf16 bf16x2_t __attribute__((ext_vector_type(2)));
#define LDS_AS __attribute__((address_space(3)))

constexpr int NB = 16, SQ = 48, DM = 448, HQ = 16, HKV = 8, SK = 48, DH = 256;
constexpr int SMEM_A = 163840;
constexpr int QSLOT = 45056;
constexpr int VIMG = 49152;
constexpr int VW_BLK = 43008;
constexpr int VW_R2 = 28672;
constexpr int P_BLK = 4608;
constexpr int P_R2 = 3072;
constexpr size_t P_OFF = (size_t)256 * VW_BLK;

__device__ __forceinline__ unsigned pack_bf16(float a, float b) {
  f32x2 v = {a, b};
  bf16x2_t r = __builtin_convertvector(v, bf16x2_t);
  return __builtin_bit_cast(unsigned, r);
}
__device__ __forceinline__ f32x4 mfma16(v4u a, v4u b, f32x4 c) {
  return __builtin_amdgcn_mfma_f32_16x16x32_bf16(__builtin_bit_cast(bf16x8_t, a), __builtin_bit_cast(bf16x8_t, b), c, 0, 0, 0);
}
__device__ __forceinline__ void swap16(float& a, float& b) { asm volatile("s_nop 1\n\tv_permlane16_swap_b32 %0, %1\n\ts_nop 1" : "+v"(a), "+v"(b)); }
__device__ __forceinline__ void swap32(float& a, float& b) { asm volatile("s_nop 1\n\tv_permlane32_swap_b32 %0, %1\n\ts_nop 1" : "+v"(a), "+v"(b)); }
__device__ __forceinline__ float xlane_max(float v) {
  float a = v, b = v;
  swap16(a, b);
  a = fmaxf(a, b); b = a;
  swap32(a, b);
  return fmaxf(a, b);
}
__device__ __forceinline__ float xlane_sum(float v) {
  float a = v, b = v;
  swap16(a, b);
  a = a + b; b = a;
  swap32(a, b);
  return a + b;
}
__device__ __forceinline__ v2u tr_read(const unsigned char* p) {
  v4s r = __builtin_amdgcn_ds_read_tr16_b64_v4i16((LDS_AS v4s*)p);
  return __builtin_bit_cast(v2u, r);
}
__device__ __forceinline__ void dma16(__amdgpu_buffer_rsrc_t rs, unsigned ldsoff, int voff, int soff) {
  asm volatile("s_nop 4\n\ts_mov_b32 m0, %0\n\ts_nop 0\n\tbuffer_load_dwordx4 %1, %2, %3 offen lds" :: "s"(ldsoff), "v"(voff), "s"(rs), "s"(soff) : "m0", "memory");
}
#define DMA16(RS, LDSOFF, VOFF, SOFF) dma16((RS), lds0 + (LDSOFF), (VOFF), (SOFF))
#define WAITV(n) asm volatile("s_waitcnt vmcnt(" #n ")" ::: "memory")
#define RBAR() { asm volatile("s_waitcnt lgkmcnt(0)" ::: "memory"); __builtin_amdgcn_sched_barrier(0); __builtin_amdgcn_s_barrier(); __builtin_amdgcn_sched_barrier(0); }

constexpr int KIMG = 2 * QSLOT;
__device__ __forceinline__ int next_slot(int cur) { return cur == 0 ? 2 * QSLOT : cur - QSLOT; }

__global__ __launch_bounds__(768, 3) void gqa_kernel_a(const unsigned char* __restrict__ x, const unsigned char* __restrict__ kc,
                                                       const unsigned char* __restrict__ vc, const unsigned char* __restrict__ wq,
                                                       const unsigned char* __restrict__ wo, unsigned char* __restrict__ ws,
                                                       const unsigned char* __restrict__ outp) {
  __shared__ __attribute__((aligned(16))) unsigned char smem[SMEM_A];
  const int bid = blockIdx.x;
  const int xcd = bid & 7, bi = bid >> 3;
  const int type = bi & 1, h = 2 * xcd + ((bi >> 1) & 1), bp = bi >> 2;
  const int gk = h >> 1;
  const int tid = threadIdx.x, lane = tid & 63;
  const int w = __builtin_amdgcn_readfirstlane(tid >> 6);
  const int lg = lane >> 4, lc = lane & 15;
  const int hi = lane >> 5;
  const int q4 = (lane & 15) >> 2, p4 = lane & 3;
  const int bb = (w >> 2) & 1;
  const int cq = w & 3;
  const unsigned lds0 = (unsigned)(size_t)(LDS_AS unsigned char*)smem;

  if (type == 0) {
    if (w >= 8) {
      const int ld = w - 8;
      const __amdgpu_buffer_rsrc_t rsw = __builtin_amdgcn_make_buffer_rsrc((void*)wq, 0, DM * HQ * DH * 2, 0x00020000);
      const __amdgpu_buffer_rsrc_t rsx = __builtin_amdgcn_make_buffer_rsrc((void*)(x + (size_t)bp * (2 * SQ * DM * 2)), 0, 2 * SQ * DM * 2, 0x00020000);
      const __amdgpu_buffer_rsrc_t rsk = __builtin_amdgcn_make_buffer_rsrc((void*)kc, 0, NB * HKV * SK * DH * 2, 0x00020000);
      int wv[4], xv[2];
#pragma unroll
      for (int v = 0; v < 4; ++v) {
        const int sw = (2 * (v & 1) + hi) | ((v >> 1) << 2);
        wv[v] = hi * 8192 + h * 512 + (((lane & 31) ^ (sw << 1)) * 16);
      }
#pragma unroll
      for (int v = 0; v < 2; ++v) xv[v] = (lane >> 3) * 896 + (((lane & 7) ^ ((4 * ((ld + v) & 1) + (lane >> 4)) & 7)) * 16);
      auto issue = [&](int cc, unsigned slot, int lo, int hi_) {
#pragma unroll
        for (int i = 0; i < 3; ++i) if (i >= lo && i < hi_) DMA16(rsx, slot + 32768 + (3 * ld + i) * 1024, xv[i & 1], (3 * ld + i) * 8 * 896 + 128 * cc);
#pragma unroll
        for (int i = 0; i < 8; ++i) if (i + 3 >= lo && i + 3 < hi_)
          DMA16(rsw, slot + ld * 8192 + i * 1024, wv[(i & 1) | (((i >> 2) & 1) << 1)], (2 * cc * 32 + 16 * ld + 2 * i) * 8192);
      };
      issue(0, 0, 0, 11);
      issue(1, 2 * QSLOT, 0, 6);
      WAITV(6); RBAR()
      issue(1, 2 * QSLOT, 6, 11);
      issue(2, QSLOT, 0, 11);
      int islot = 0;
#pragma unroll 1
      for (int c = 1; c < 5; ++c) {
        WAITV(11); RBAR()
        issue(c + 2, islot, 0, 11);
        islot = next_slot(islot);
      }
      WAITV(11);
      RBAR()
      {
#pragma unroll 1
        for (int ii = 0; ii < 12; ++ii) {
          const int i = 12 * (ld & 1) + ii;
          const int kvo = hi * 512 + (((lane & 31) ^ ((2 * i + hi) & 15)) * 16);
          DMA16(rsk, KIMG + (ld >> 1) * 24576 + i * 1024, kvo, ((2 * bp + (ld >> 1)) * HKV + gk) * (SK * DH * 2) + i * 1024);
        }
      }
      WAITV(12);
      RBAR()
      WAITV(0);
      RBAR()
      RBAR()
    } else {
      f32x4 acc[3][4];
#pragma unroll
      for (int qt = 0; qt < 3; ++qt)
#pragma unroll
        for (int j = 0; j < 4; ++j) acc[qt][j] = f32x4{0.f, 0.f, 0.f, 0.f};
      const int xrd = 32768 + (48 * bb + lc) * 128 + ((lg ^ ((lc >> 1) & 7)) * 16);
      const int srd = q4 | ((lg & 1) << 2);
      const int wrd = (8 * lg + q4) * 512 + (((8 * cq + (p4 >> 1)) ^ (srd << 1)) * 16) + (p4 & 1) * 8;
      int cslot = 0;
#define ILQ(n) _Pragma("unroll") for (int i_ = 0; i_ < (n); ++i_) { __builtin_amdgcn_sched_group_barrier(0x8, 1, 0); __builtin_amdgcn_sched_group_barrier(0x100, 1, 0); }
      v4u xa[3], xb2[3], wa[4], wb[4], ka[3][2];
      auto rd = [&](v4u (&xf)[3], v4u (&wf)[4], const unsigned char* slot, int ss) {
#pragma unroll
        for (int qt = 0; qt < 3; ++qt) xf[qt] = *(const v4u*)(slot + ((xrd ^ (64 * ss)) + qt * 2048));
#pragma unroll
        for (int j = 0; j < 4; ++j) {
          v2u a = tr_read(slot + ss * 16384 + (wrd ^ (32 * j)));
          v2u b = tr_read(slot + ss * 16384 + (wrd ^ (32 * j)) + 2048);
          wf[j] = v4u{a[0], a[1], b[0], b[1]};
        }
      };
      auto mm = [&](const v4u (&xf)[3], const v4u (&wf)[4]) {
#pragma unroll
        for (int j = 0; j < 4; ++j)
#pragma unroll
          for (int qt = 0; qt < 3; ++qt) acc[qt][j] = mfma16(wf[j], xf[qt], acc[qt][j]);
      };
      unsigned tw = 0;
      RBAR()
      rd(xa, wa, smem, 0);
#pragma unroll
      for (int c = 0; c < 7; ++c) {
        const unsigned char* slot = smem + cslot;
        rd(xb2, wb, slot, 1);
        mm(xa, wa);
        ILQ(11)
        __builtin_amdgcn_sched_barrier(0);
        cslot = next_slot(cslot);
        RBAR()
        if (c == 1) {
          const unsigned char* ta;
          if (w < 2)      ta = kc + (size_t)((2 * bp + w) * HKV + gk) * (SK * DH * 2);
          else if (w < 4) ta = ws + P_OFF + (size_t)((2 * bp + (w - 2)) * 16 + h) * P_BLK;
          else            ta = outp + (size_t)(xcd + 8 * (w & 1)) * (SQ * DM * 2) + (bi & 15) * 2688;
          tw = __builtin_amdgcn_raw_buffer_load_b32(__builtin_amdgcn_make_buffer_rsrc((void*)ta, 0, 64, 0x00020000), 0, 0, 0);
          __builtin_amdgcn_sched_barrier(0);
        }
        if (c < 6) rd(xa, wa, smem + cslot, 0);
        else {
          const unsigned char* kl = smem + KIMG + (48 * bb + lc) * 512 + (lg & 1) * 8;
#pragma unroll
          for (int kt = 0; kt < 3; ++kt)
#pragma unroll
            for (int f = 0; f < 2; ++f) {
              v2u a = *(const v2u*)(kl + kt * 8192 + (((8 * cq + 4 * f + (lg >> 1)) ^ lc) * 16));
              v2u b = *(const v2u*)(kl + kt * 8192 + (((8 * cq + 4 * f + 2 + (lg >> 1)) ^ lc) * 16));
              ka[kt][f] = v4u{a[0], a[1], b[0], b[1]};
            }
        }
        mm(xb2, wb);
        ILQ(11)
        __builtin_amdgcn_sched_barrier(0);
      }
      f32x4 sacc[3][3];
      {
#pragma unroll
        for (int qt = 0; qt < 3; ++qt) {
#pragma unroll
          for (int kt = 0; kt < 3; ++kt) sacc[kt][qt] = f32x4{0.f, 0.f, 0.f, 0.f};
#pragma unroll
          for (int f = 0; f < 2; ++f) {
            v4u qf;
            qf[0] = pack_bf16(acc[qt][2 * f][0], acc[qt][2 * f][1]);
            qf[1] = pack_bf16(acc[qt][2 * f][2], acc[qt][2 * f][3]);
            qf[2] = pack_bf16(acc[qt][2 * f + 1][0], acc[qt][2 * f + 1][1]);
            qf[3] = pack_bf16(acc[qt][2 * f + 1][2], acc[qt][2 * f + 1][3]);
#pragma unroll
            for (int kt = 0; kt < 3; ++kt) sacc[kt][qt] = mfma16(ka[kt][f], qf, sacc[kt][qt]);
          }
        }
      }
      asm volatile("" :: "v"(tw));
      unsigned char* sp = smem;
#pragma unroll
      for (int kt = 0; kt < 3; ++kt)
#pragma unroll
        for (int qt = 0; qt < 3; ++qt) *(f32x4*)(sp + ((w * 9 + qt * 3 + kt) * 64 + lane) * 16) = sacc[kt][qt];
      RBAR()
      if (w < 6) {
        const int b2 = w >= 3 ? 1 : 0, qt = w - 3 * b2;
        f32x4 pr[4][3];
        const unsigned char* spl = sp + ((36 * b2 + qt * 3) * 64 + lane) * 16;
#pragma unroll
        for (int wv = 0; wv < 4; ++wv)
#pragma unroll
          for (int kt = 0; kt < 3; ++kt) pr[wv][kt] = *(const f32x4*)(spl + (wv * 9 + kt) * 1024);
        __builtin_amdgcn_sched_barrier(0);
        float sv[12];
#pragma unroll
        for (int kt = 0; kt < 3; ++kt) {
          const f32x4 a = (pr[0][kt] + pr[1][kt]) + (pr[2][kt] + pr[3][kt]);
          const unsigned u0 = pack_bf16(a[0], a[1]), u1 = pack_bf16(a[2], a[3]);
          sv[4 * kt + 0] = __builtin_bit_cast(float, u0 << 16);
          sv[4 * kt + 1] = __builtin_bit_cast(float, u0 & 0xffff0000u);
          sv[4 * kt + 2] = __builtin_bit_cast(float, u1 << 16);
          sv[4 * kt + 3] = __builtin_bit_cast(float, u1 & 0xffff0000u);
        }
        float m = fmaxf(fmaxf(fmaxf(sv[0], sv[1]), fmaxf(sv[2], sv[3])), fmaxf(fmaxf(sv[4], sv[5]), fmaxf(sv[6], sv[7])));
        m = fmaxf(m, fmaxf(fmaxf(sv[8], sv[9]), fmaxf(sv[10], sv[11])));
        m = xlane_max(m);
        const float cs = 0.0625f * 1.44269504088896340736f;
        const float mc = -m * cs;
        float sum = 0.f;
#pragma unroll
        for (int i = 0; i < 12; ++i) {
          sv[i] = __builtin_amdgcn_exp2f(__builtin_fmaf(sv[i], cs, mc));
          sum += sv[i];
        }
        sum = xlane_sum(sum);
        const float inv = __builtin_amdgcn_rcpf(sum);
        v4u p1;
        v2u p2;
        p1[0] = pack_bf16(sv[0] * inv, sv[1] * inv);
        p1[1] = pack_bf16(sv[2] * inv, sv[3] * inv);
        p1[2] = pack_bf16(sv[4] * inv, sv[5] * inv);
        p1[3] = pack_bf16(sv[6] * inv, sv[7] * inv);
        p2[0] = pack_bf16(sv[8] * inv, sv[9] * inv);
        p2[1] = pack_bf16(sv[10] * inv, sv[11] * inv);
        unsigned char* pb = ws + P_OFF + (size_t)((2 * bp + b2) * 16 + h) * P_BLK;
        *(v4u*)(pb + qt * 1024 + lane * 16) = p1;
        *(v2u*)(pb + P_R2 + qt * 512 + lane * 8) = p2;
      }
    }
  } else {
    if (w >= 8) {
      const int ld = w - 8;
      const __amdgpu_buffer_rsrc_t rs = __builtin_amdgcn_make_buffer_rsrc((void*)wo, 0, DM * HQ * DH * 2, 0x00020000);
      const __amdgpu_buffer_rsrc_t rsv = __builtin_amdgcn_make_buffer_rsrc((void*)vc, 0, NB * HKV * SK * DH * 2, 0x00020000);
      int vv[2];
#pragma unroll
      for (int v = 0; v < 2; ++v) vv[v] = (lane >> 3) * 512 + (((lane & 7) ^ ((4 * ((ld + v) & 1) + (lane >> 4)) & 7)) * 16);
      int wo_voff[7];
#pragma unroll
      for (int r = 0; r < 7; ++r) {
        const int P = 1024 * r + 16 * lane;
        const int row = P / 896;
        const int phys = (P - 896 * row) >> 4;
        const int sp_ = ((row >> 1) & 1) | ((ld & 1) << 1);
        wo_voff[r] = row * 896 + ((phys ^ (sp_ << 1)) * 16);
      }
      auto issue = [&](int st) {
        if ((st & 1) == 0) {
#pragma unroll
          for (int i = 0; i < 3; ++i)
            DMA16(rsv, (st >> 1) * 12288 + (3 * ld + i) * 1024, vv[i & 1],
                  ((2 * bp + (ld >> 1)) * HKV + gk) * (SK * DH * 2) + (3 * (ld & 1) + i) * 8 * 512 + 64 * st);
        }
        const unsigned slot = VIMG + (st & 3) * 28672 + ld * 7168;
#pragma unroll
        for (int r = 0; r < 7; ++r) DMA16(rs, slot + r * 1024, wo_voff[r], (h * 256 + 32 * st + 8 * ld) * 896);
      };
      __builtin_amdgcn_s_sleep(30);
      issue(0); issue(1);
      WAITV(7);  RBAR() issue(2); issue(3);
      WAITV(17); RBAR() issue(4);
      WAITV(17); RBAR() issue(5);
      WAITV(17); RBAR() issue(6);
      WAITV(17); RBAR() issue(7);
      WAITV(17); RBAR()
      WAITV(7);  RBAR()
      WAITV(0);  RBAR()
    } else {
      const int cw = cq;
      f32x4 acc[3][7];
#pragma unroll
      for (int kt = 0; kt < 3; ++kt)
#pragma unroll
        for (int ct = 0; ct < 7; ++ct) acc[kt][ct] = f32x4{0.f, 0.f, 0.f, 0.f};
      const int vrd = (48 * bb + lc) * 128 + ((lg ^ ((lc >> 1) & 7)) * 16);
      const int sp2 = ((q4 >> 1) & 1) | ((lg & 1) << 1);
      const int wrow = (8 * lg + q4) * 896 + (p4 & 1) * 8;
      v4u va[3], vb[3], wa[4], wb[3];
      auto rdv = [&](v4u (&vf)[3], int c, int ss) {
#pragma unroll
        for (int kt = 0; kt < 3; ++kt) vf[kt] = *(const v4u*)(smem + c * 12288 + ((vrd ^ (64 * ss)) + kt * 2048));
      };
      int wbase[4];
#pragma unroll
      for (int i = 0; i < 4; ++i) wbase[i] = VIMG + wrow + (((14 * cw + 2 * i + (p4 >> 1)) ^ (sp2 << 1)) * 16);
      auto rdw = [&](v4u* wf, int st, int ct0, int n) {
#pragma unroll
        for (int i = 0; i < n; ++i) {
          const unsigned char* p = smem + wbase[i] + ((st & 3) * 28672 + (ct0 ? 128 : 0));
          v2u a = tr_read(p);
          v2u b = tr_read(p + 3584);
          wf[i] = v4u{a[0], a[1], b[0], b[1]};
        }
      };
      auto mm = [&](const v4u (&vf)[3], const v4u* wf, int ct0, int n) {
#pragma unroll
        for (int i = 0; i < n; ++i)
#pragma unroll
          for (int kt = 0; kt < 3; ++kt) acc[kt][ct0 + i] = mfma16(vf[kt], wf[i], acc[kt][ct0 + i]);
      };
#define SB __builtin_amdgcn_sched_barrier(0);
#define IL(n) _Pragma("unroll") for (int i_ = 0; i_ < (n); ++i_) { __builtin_amdgcn_sched_group_barrier(0x8, 1, 0); __builtin_amdgcn_sched_group_barrier(0x100, 1, 0); }
#define IL2(n) _Pragma("unroll") for (int i_ = 0; i_ < (n); ++i_) { __builtin_amdgcn_sched_group_barrier(0x8, 1, 0); __builtin_amdgcn_sched_group_barrier(0x100, 2, 0); }
      unsigned tw = 0;
      RBAR()
      rdv(va, 0, 0);
      rdw(wa, 0, 0, 4);
#pragma unroll
      for (int c = 0; c < 4; ++c) {
        rdw(wb, 2 * c, 4, 3);
        mm(va, wa, 0, 4); IL(6) SB
        RBAR()
        if (c == 1) {
          tw = __builtin_amdgcn_raw_buffer_load_b32(__builtin_amdgcn_make_buffer_rsrc((void*)(ws + (size_t)((2 * bp + bb) * 16 + h) * VW_BLK + cw * 10752), 0, 64, 0x00020000), 0, 0, 0);
          __builtin_amdgcn_sched_barrier(0);
        }
        rdv(vb, c, 1); rdw(wa, 2 * c + 1, 0, 4);
        mm(va, wb, 4, 3); IL2(6) SB
        rdw(wb, 2 * c + 1, 4, 3);
        mm(vb, wa, 0, 4); IL(6) SB
        if (c < 3) {
          RBAR()
          rdv(va, c + 1, 0);
          rdw(wa, 2 * c + 2, 0, 4);
          mm(vb, wb, 4, 3); IL2(6) SB
        } else {
          mm(vb, wb, 4, 3); SB
        }
      }
      asm volatile("" :: "v"(tw));
      __builtin_amdgcn_s_sleep(30);
      const __amdgpu_buffer_rsrc_t rso = __builtin_amdgcn_make_buffer_rsrc((void*)ws, 0, 256 * VW_BLK, 0x00020000);
      {
        const int ob = ((2 * bp + bb) * 16 + h) * VW_BLK;
        const int o1 = ob + 7 * cw * 1024 + lane * 16;
        const int o2 = ob + VW_R2 + 7 * cw * 512 + lane * 8;
#pragma unroll
        for (int ct = 0; ct < 7; ++ct) {
          v4u o;
          o[0] = pack_bf16(acc[0][ct][0], acc[0][ct][1]);
          o[1] = pack_bf16(acc[0][ct][2], acc[0][ct][3]);
          o[2] = pack_bf16(acc[1][ct][0], acc[1][ct][1]);
          o[3] = pack_bf16(acc[1][ct][2], acc[1][ct][3]);
          __builtin_amdgcn_raw_buffer_store_b128(o, rso, o1 + ct * 1024, 0, 16  );
          v2u o3;
          o3[0] = pack_bf16(acc[2][ct][0], acc[2][ct][1]);
          o3[1] = pack_bf16(acc[2][ct][2], acc[2][ct][3]);
          __builtin_amdgcn_raw_buffer_store_b64(o3, rso, o2 + ct * 512, 0, 16  );
        }
      }
    }
  }
}

__global__ __launch_bounds__(512) void gqa_kernel_b(const unsigned char* __restrict__ ws, unsigned char* __restrict__ out) {
  __shared__ __attribute__((aligned(16))) float red[8 * 48 * 36];
  const int bid = blockIdx.x;
  const int xcd = bid & 7, bi = bid >> 3;
  const int b = xcd + 8 * (bi / 14), m = bi % 14;
  const int tid = threadIdx.x, lane = tid & 63;
  const int w = __builtin_amdgcn_readfirstlane(tid >> 6);
  const int lg = lane >> 4, lc = lane & 15;

  v4u p1[2][3], v1[2][2];
  v2u p2[2][3], v2[2][2];
#pragma unroll
  for (int hh = 0; hh < 2; ++hh) {
    const int h = 2 * w + hh;
    const unsigned char* pb = ws + P_OFF + (size_t)(b * 16 + h) * P_BLK;
    const unsigned char* vb = ws + (size_t)(b * 16 + h) * VW_BLK;
#pragma unroll
    for (int ct = 0; ct < 2; ++ct) {
      v1[hh][ct] = *(const v4u*)(vb + (2 * m + ct) * 1024 + lane * 16);
      v2[hh][ct] = *(const v2u*)(vb + VW_R2 + (2 * m + ct) * 512 + lane * 8);
    }
#pragma unroll
    for (int qt = 0; qt < 3; ++qt) {
      p1[hh][qt] = *(const v4u*)(pb + qt * 1024 + lane * 16);
      p2[hh][qt] = *(const v2u*)(pb + P_R2 + qt * 512 + lane * 8);
    }
  }
  __builtin_amdgcn_sched_barrier(0);
#pragma unroll
  for (int qt = 0; qt < 3; ++qt)
#pragma unroll
    for (int ct = 0; ct < 2; ++ct) {
      f32x4 a = f32x4{0.f, 0.f, 0.f, 0.f};
      a = mfma16(p1[0][qt], v1[0][ct], a);
      a = mfma16(p1[1][qt], v1[1][ct], a);
      v4u pa = {p2[0][qt][0], p2[0][qt][1], p2[1][qt][0], p2[1][qt][1]};
      v4u va = {v2[0][ct][0], v2[0][ct][1], v2[1][ct][0], v2[1][ct][1]};
      a = mfma16(pa, va, a);
#pragma unroll
      for (int r = 0; r < 4; ++r) red[(w * 48 + 16 * qt + 4 * lg + r) * 36 + 16 * ct + lc] = a[r];
    }
  __syncthreads();
  if (tid < 384) {
    const int row = tid >> 3, c4 = tid & 7;
    f32x4 s = *(const f32x4*)(red + row * 36 + 4 * c4);
#pragma unroll
    for (int wv = 1; wv < 8; ++wv) s += *(const f32x4*)(red + (wv * 48 + row) * 36 + 4 * c4);
    v2u o = {pack_bf16(s[0], s[1]), pack_bf16(s[2], s[3])};
    *(v2u*)(out + (((size_t)b * SQ + row) * DM + 32 * m + 4 * c4) * 2) = o;
  }
}

extern "C" void kernel_launch(void* const* d_in, const int* in_sizes, int n_in, void* d_out, int out_size, void* d_ws,
                              size_t ws_size, hipStream_t stream) {
  const unsigned char* x = (const unsigned char*)d_in[0];
  const unsigned char* kc = (const unsigned char*)d_in[1];
  const unsigned char* vc = (const unsigned char*)d_in[2];
  const unsigned char* wq = (const unsigned char*)d_in[3];
  const unsigned char* wo = (const unsigned char*)d_in[4];
  unsigned char* ws = (unsigned char*)d_ws;
  gqa_kernel_a<<<256, 768, 0, stream>>>(x, kc, vc, wq, wo, ws, (const unsigned char*)d_out);
  gqa_kernel_b<<<224, 512, 0, stream>>>(ws, (unsigned char*)d_out);
}
```
